```python
import torch

def _softmax(x, axis=-1):
    return torch.softmax(x, dim=axis)
def _gelu(x):
    return torch.nn.functional.gelu(x, approximate="tanh")
def _silu(x):
    return torch.nn.functional.silu(x)
def _sigmoid(x):
    return torch.sigmoid(x)
def _layernorm(x, axis=-1, eps=1e-5):
    m = x.mean(dim=axis, keepdim=True)
    v = x.var(dim=axis, keepdim=True, unbiased=False)
    return (x - m) / torch.sqrt(v + eps)
def _scatter_add(tgt, idx, src, axis=0):
    return tgt.clone().index_add_(axis, idx, src)
def _segment_sum(src, seg, n):
    out = torch.zeros((n,) + src.shape[1:], dtype=src.dtype)
    return out.index_add_(0, seg, src)
def _pad(x, pw):
    flat = tuple(v for pair in reversed(pw) for v in pair)
    return torch.nn.functional.pad(x, flat)

kernel_name = 'standalone__softmax_full_bidir__decode'

def setup_inputs():

    _w = (lambda a: (lambda t: t.to(torch.bfloat16) if t.is_floating_point() else t)(torch.as_tensor(a)))
    B, H, Sq, Skv, Dh = 1, 32, 1, 4096, 128
    return {
        "q": _w((lambda *s: torch.randn(*s, dtype=torch.bfloat16) * 0.8)(B, H, Sq, Dh)),
        "k": _w((lambda *s: torch.randn(*s, dtype=torch.bfloat16) * 0.8)(B, H, Skv, Dh)),
        "v": _w((lambda *s: torch.randn(*s, dtype=torch.bfloat16) * 0.05)(B, H, Skv, Dh)),
    }

def reference(q, k, v):
    scale = q.shape[-1] ** -0.5
    scores = torch.einsum('bhqd,bhkd->bhqk', q, k) * scale
    attn = _softmax(scores, axis=-1)
    return torch.einsum('bhqk,bhkd->bhqd', attn, v)
```

```cpp
#include <hip/hip_runtime.h>
#include <hip/hip_bf16.h>

constexpr int PACE_NS = 270;
constexpr int SWEEP_T = 124;
#define SWEEP_D "6"

constexpr int H = 32, D = 128, NSPLIT = 8, W = 16;

using bf16x2 = __attribute__((ext_vector_type(2))) __bf16;
using bf16x8 = __attribute__((ext_vector_type(8))) __bf16;
using u32x4 = __attribute__((ext_vector_type(4))) unsigned;
using f32x4 = __attribute__((ext_vector_type(4))) float;
using f32x2 = __attribute__((ext_vector_type(2))) float;
using u64 = unsigned long long;
using u64x4 = __attribute__((ext_vector_type(4))) u64;
typedef __attribute__((address_space(1))) unsigned char gu8;
typedef __attribute__((address_space(1))) unsigned gu32;
typedef __attribute__((address_space(1))) u64 gu64;
typedef __attribute__((address_space(1))) u32x4 gu32x4;
#define RLX_AGENT __ATOMIC_RELAXED, __HIP_MEMORY_SCOPE_AGENT
#define SB() __builtin_amdgcn_sched_barrier(0)

__device__ __forceinline__ float dot2(unsigned k, unsigned q, float acc) {
  return __builtin_amdgcn_fdot2_f32_bf16(__builtin_bit_cast(bf16x2, k), __builtin_bit_cast(bf16x2, q), acc, false);
}
__device__ __forceinline__ unsigned cvtpk(float lo, float hi) {
  bf16x2 r = {(__bf16)lo, (__bf16)hi};
  return __builtin_bit_cast(unsigned, r);
}
template <int CTRL> __device__ __forceinline__ float dpp(float x) {
  return __builtin_bit_cast(float, __builtin_amdgcn_mov_dpp(__builtin_bit_cast(int, x), CTRL, 0xf, 0xf, true));
}
__device__ __forceinline__ float row_sum(float x) {
  x += dpp<0xB1>(x); x += dpp<0x4E>(x); x += dpp<0x141>(x); x += dpp<0x140>(x); return x;
}
__device__ __forceinline__ float xrow_max(float x) {
  auto s = __builtin_amdgcn_permlane16_swap(__float_as_uint(x), __float_as_uint(x), false, false);
  x = fmaxf(__uint_as_float(s[0]), __uint_as_float(s[1]));
  auto t = __builtin_amdgcn_permlane32_swap(__float_as_uint(x), __float_as_uint(x), false, false);
  return fmaxf(__uint_as_float(t[0]), __uint_as_float(t[1]));
}
__device__ __forceinline__ float xrow_sum(float x) {
  auto s = __builtin_amdgcn_permlane16_swap(__float_as_uint(x), __float_as_uint(x), false, false);
  x = __uint_as_float(s[0]) + __uint_as_float(s[1]);
  auto t = __builtin_amdgcn_permlane32_swap(__float_as_uint(x), __float_as_uint(x), false, false);
  return __uint_as_float(t[0]) + __uint_as_float(t[1]);
}
__device__ __forceinline__ unsigned now() { return (unsigned)__builtin_amdgcn_s_memrealtime(); }

constexpr unsigned HEAD_B = 16384, SLOTS_B = 1024, SLOT_B = 640;

__global__ void __launch_bounds__(W * 64, W / 4)
attn_decode(const void* q_, const void* k_, const void* v_, void* out_, void* ws_) {
  __shared__ __attribute__((aligned(16))) float lds[W * D + 2 * W];
  u64x4 a03;
  u64 a4;
  asm volatile("s_load_dwordx8 %0, %2, 0x0\n\ts_load_dwordx2 %1, %2, 0x20"
               : "=&s"(a03), "=&s"(a4) : "s"(__builtin_amdgcn_kernarg_segment_ptr()));
  SB();
  const int b = blockIdx.x;
  const int h = (b & 7) * 4 + ((b >> 3) & 3), s = b >> 5;
  const int tid = threadIdx.x, lane = tid & 63, wave = __builtin_amdgcn_readfirstlane(tid >> 6);
  const int c = lane & 15;
  constexpr float C2 = 0.088388347648318440f * 1.4426950408889634f;

  unsigned so = ((unsigned)h << 20) + (((wave + h) & 15) << 16) + (((s + wave) & 7) << 13) + 4096 + lane * 16;
  unsigned vq = h * 256 + c * 16, vep = h * HEAD_B;
  asm volatile("" : "+v"(so), "+v"(vq), "+v"(vep));
  SB();
  u64 t64;
  asm volatile("s_waitcnt lgkmcnt(0)\n\ts_memrealtime s[40:41]" : "+s"(a03), "+s"(a4), "=&{s[40:41]}"(t64));
  gu8 *const q = (gu8*)a03[0], *const k = (gu8*)a03[1], *const v = (gu8*)a03[2], *const out = (gu8*)a03[3], *const ws = (gu8*)a4;
  const u32x4 qf = *(const gu32x4*)(q + vq);
  const unsigned e0 = __hip_atomic_load((gu32*)(ws + vep), RLX_AGENT);
  SB();
  asm volatile("s_cmp_eq_u32 %[g], 0\n\t"
               "s_cbranch_scc1 2f\n\t"
               "1:\n\t"
               "s_sleep 1\n\t"
               "s_memrealtime s[42:43]\n\t"
               "s_waitcnt lgkmcnt(0)\n\t"
               "s_sub_u32 s42, s42, s40\n\t"
               "s_cmp_lt_i32 s42, %[g]\n\t"
               "s_cbranch_scc1 1b\n\t"
               "2:"
               : "+{s[40:41]}"(t64) : [g] "s"((wave * (PACE_NS * 1024 / 10)) >> 10) : "scc", "s42", "s43");
#define LD(dst, p) _Pragma("unroll") for (int i = 0; i < 8; ++i) { \
    dst[i] = __builtin_nontemporal_load((const gu32x4*)(p + (size_t)so + (i * 1024 - 4096))); SB(); }
  u32x4 kr[8], vr[8], pf[8];
  SB();
  LD(kr, k);
  asm volatile("s_waitcnt lgkmcnt(0)" : "+{s[40:41]}"(t64));
  const unsigned t0 = (unsigned)t64;
  gu8* hw = ws + h * HEAD_B;
  unsigned tag = e0 + 0x9E3779B9u;
  gu64* mine = (gu64*)(hw + SLOTS_B + (((tid >> 7) & 1) * NSPLIT + s) * SLOT_B) + (tid & 127);
  const bool ko = tid < 256 && (tid & 127) < 66;
  if (ko) __hip_atomic_store(mine, (u64)e0 << 32, RLX_AGENT);
  SB();
  if (wave >= W / 2) __builtin_amdgcn_s_barrier();
  SB();

  float x[8];
#pragma unroll
  for (int i = 0; i < 8; ++i) {
    float d = dot2(kr[i][0], qf[0], 0.f);
    d = dot2(kr[i][1], qf[1], d); d = dot2(kr[i][2], qf[2], d); d = dot2(kr[i][3], qf[3], d);
    x[i] = row_sum(d) * C2;
  }
  float m = x[0], l = 0.f;
#pragma unroll
  for (int i = 1; i < 8; ++i) m = fmaxf(m, x[i]);
  m = xrow_max(m);
  const unsigned mlo = c < 8 ? 0u : 0xffff0000u;
  const unsigned half = (c & 1) ? 0xffff0000u : 0x0000ffffu;
  const int dsel = (c & 7) >> 1;
  const unsigned mk0 = dsel == 0 ? half : 0u, mk1 = dsel == 1 ? half : 0u, mk2 = dsel == 2 ? half : 0u, mk3 = dsel == 3 ? half : 0u;
#pragma unroll
  for (int i = 0; i < 8; ++i) {
    const float p = __builtin_amdgcn_exp2f(x[i] - m);
    l += p;
    const unsigned hi = cvtpk(p, p);
    const float r = p - __uint_as_float(hi & mlo);
    const unsigned pk = cvtpk(r, r);
    pf[i] = u32x4{pk & mk0, pk & mk1, pk & mk2, pk & mk3};
    asm volatile("" : "+v"(pf[i]));
  }
  u64 pre = 0;
  if (ko) pre = __hip_atomic_load(mine, RLX_AGENT);
  if (wave < W / 2) __builtin_amdgcn_s_barrier();
  SB();
  {
    const int g = (W + wave) * PACE_NS / 10;
    while ((int)(now() - t0) < g - 36) __builtin_amdgcn_s_sleep(8);
    while ((int)(now() - t0) < g) __builtin_amdgcn_s_sleep(1);
    SB();
  }
  LD(vr, v);
  l = xrow_sum(l);
  *reinterpret_cast<f32x2*>(lds + W * D + 2 * wave) = f32x2{m, l};
  const gu8* sb = hw + SLOTS_B + (wave & 1) * (NSPLIT * SLOT_B) + 4 * SLOT_B;
  unsigned vo = lane * 8, vme = s * SLOT_B + lane * 8;
  unsigned vml = ((c & 7) == 0 ? 1 : (c & 7)) * SLOT_B + 512 + (c >> 3) * 8;
  unsigned vmy = s * SLOT_B + 512 + (c >> 3) * 8;
  const unsigned lbase = (unsigned)(size_t)(__attribute__((address_space(3))) float*)lds;
  unsigned vrec = lbase + ((wave & 1) * 64 + lane) * 4, vl = lbase + (W * D + 2 * c) * 4, vout = h * 256 + (tid & 127) * 2;
  const u64 own = __ballot((c & 7) == 0), hi8 = __ballot(c >= 8);
  unsigned vw = lbase + (wave * D + 8 * c + 4 * ((lane >> 4) & 1) + 2 * (lane >> 5)) * 4;
  asm volatile("" : "+v"(vo), "+v"(vme), "+v"(vml), "+v"(vmy), "+v"(vrec), "+v"(vl), "+v"(vout), "+v"(vw), "+v"(tag)
               : "s"(out), "s"(sb), "s"(own), "s"(hi8));
  SB();
  f32x4 acc = {0.f, 0.f, 0.f, 0.f};
#pragma unroll
  for (int i = 0; i < 8; ++i)
    acc = __builtin_amdgcn_mfma_f32_16x16x32_bf16(__builtin_bit_cast(bf16x8, pf[i]), __builtin_bit_cast(bf16x8, vr[i]), acc, 0, 0, 0);
  auto s02 = __builtin_amdgcn_permlane32_swap(__float_as_uint(acc[0]), __float_as_uint(acc[2]), false, false);
  auto s13 = __builtin_amdgcn_permlane32_swap(__float_as_uint(acc[1]), __float_as_uint(acc[3]), false, false);
  const f32x2 o2 = {__uint_as_float(s02[0]) + __uint_as_float(s02[1]), __uint_as_float(s13[0]) + __uint_as_float(s13[1])};
  *(__attribute__((address_space(3))) f32x2*)(size_t)vw = o2;
  asm volatile("" :: "v"(pre));
  asm volatile("s_waitcnt vmcnt(0) lgkmcnt(0)\n\ts_barrier" ::: "memory");
  if (wave == 2 && b == 0) { const unsigned n0 = now(); const int sk = (__builtin_ctzll(a03[1])) * 100; while ((int)(now() - n0) < sk) __builtin_amdgcn_s_sleep(2); }
  if (wave >= 2) return;

#define DPPM " row_mask:0xf bank_mask:0xf\n\t"
  static_assert(SLOT_B == 640 && W == 16 && NSPLIT == 8, "offsets in the asm");
  asm volatile(
      "ds_read_b64 v[26:27], %[vl]\n\t"
      "ds_read2st64_b32 v[0:1], %[vrec] offset1:2\n\t"
      "ds_read2st64_b32 v[2:3], %[vrec] offset0:4 offset1:6\n\t"
      "ds_read2st64_b32 v[4:5], %[vrec] offset0:8 offset1:10\n\t"
      "ds_read2st64_b32 v[6:7], %[vrec] offset0:12 offset1:14\n\t"
      "ds_read2st64_b32 v[8:9], %[vrec] offset0:16 offset1:18\n\t"
      "ds_read2st64_b32 v[10:11], %[vrec] offset0:20 offset1:22\n\t"
      "ds_read2st64_b32 v[12:13], %[vrec] offset0:24 offset1:26\n\t"
      "ds_read2st64_b32 v[14:15], %[vrec] offset0:28 offset1:30\n\t"
      "s_cmp_eq_u32 %[s], 0\n\t"
      "s_waitcnt lgkmcnt(8)\n\t"
      "v_max_f32_dpp v28, v26, v26 quad_perm:[1,0,3,2]" DPPM
      "v_mov_b32 v25, %[tag]\n\t"
      "v_mov_b32 v29, %[tag]\n\t"
      "v_max_f32_dpp v28, v28, v28 quad_perm:[2,3,0,1]" DPPM
      "v_mov_b32 v30, 0\n\t"
      "s_nop 0\n\t"
      "v_max_f32_dpp v28, v28, v28 row_half_mirror" DPPM
      "s_nop 1\n\t"
      "v_max_f32_dpp v28, v28, v28 row_mirror" DPPM
      "v_sub_f32 v26, v26, v28\n\t"
      "v_exp_f32 v26, v26\n\t"
      "s_waitcnt lgkmcnt(0)\n\t"
      "v_mul_f32 v27, v26, v27\n\t"
      "v_mul_f32_dpp v16, v26, v0 row_newbcast:0" DPPM
      "v_mul_f32_dpp v17, v26, v1 row_newbcast:1" DPPM
      "v_add_f32_dpp v27, v27, v27 quad_perm:[1,0,3,2]" DPPM
      "v_mul_f32_dpp v18, v26, v2 row_newbcast:2" DPPM
      "v_mul_f32_dpp v19, v26, v3 row_newbcast:3" DPPM
      "v_add_f32_dpp v27, v27, v27 quad_perm:[2,3,0,1]" DPPM
      "v_fmac_f32_dpp v16, v26, v4 row_newbcast:4" DPPM
      "v_fmac_f32_dpp v17, v26, v5 row_newbcast:5" DPPM
      "v_fmac_f32_dpp v18, v26, v6 row_newbcast:6" DPPM
      "v_fmac_f32_dpp v19, v26, v7 row_newbcast:7" DPPM
      "v_add_f32_dpp v27, v27, v27 row_half_mirror" DPPM
      "v_fmac_f32_dpp v16, v26, v8 row_newbcast:8" DPPM
      "v_fmac_f32_dpp v17, v26, v9 row_newbcast:9" DPPM
      "v_fmac_f32_dpp v18, v26, v10 row_newbcast:10" DPPM
      "v_fmac_f32_dpp v19, v26, v11 row_newbcast:11" DPPM
      "v_add_f32_dpp v27, v27, v27 row_mirror" DPPM
      "v_fmac_f32_dpp v16, v26, v12 row_newbcast:12" DPPM
      "v_fmac_f32_dpp v17, v26, v13 row_newbcast:13" DPPM
      "v_fmac_f32_dpp v18, v26, v14 row_newbcast:14" DPPM
      "v_fmac_f32_dpp v19, v26, v15 row_newbcast:15" DPPM
      "v_pk_add_f32 v[16:17], v[16:17], v[18:19]\n\t"
      "v_cndmask_b32 v28, v28, v27, %[hi8]\n\t"
      "v_add_f32 v24, v16, v17\n\t"
      "s_cbranch_scc1 2f\n\t"
      "global_store_dwordx2 %[vme], v[24:25], %[sb] offset:-2560 sc1\n\t"
      "global_store_dwordx2 %[vmy], v[28:29], %[sb] offset:-2560 sc1\n\t"
      "s_endpgm\n\t"
      "2:\n\t"
      "s_mov_b32 s44, 0x200000\n\t"
      "s_sleep " SWEEP_D "\n\t"
      "4:\n\t"
      "s_memrealtime s[42:43]\n\t"
      "s_waitcnt lgkmcnt(0)\n\t"
      "s_sub_u32 s42, s42, %[t0]\n\t"
      "s_cmpk_lt_i32 s42, %c[tx]\n\t"
      "s_cbranch_scc1 4b\n\t"
      "1:\n\t"
      "global_load_dwordx2 v[2:3], %[vo], %[sb] offset:-1920 sc1\n\t"
      "global_load_dwordx2 v[4:5], %[vo], %[sb] offset:-1280 sc1\n\t"
      "global_load_dwordx2 v[6:7], %[vo], %[sb] offset:-640 sc1\n\t"
      "global_load_dwordx2 v[8:9], %[vo], %[sb] sc1\n\t"
      "global_load_dwordx2 v[10:11], %[vo], %[sb] offset:640 sc1\n\t"
      "global_load_dwordx2 v[12:13], %[vo], %[sb] offset:1280 sc1\n\t"
      "global_load_dwordx2 v[14:15], %[vo], %[sb] offset:1920 sc1\n\t"
      "global_load_dwordx2 v[0:1], %[vml], %[sb] offset:-2560 sc1\n\t"
      "s_waitcnt vmcnt(0)\n\t"
      "v_cndmask_b32 v20, v0, v28, %[own]\n\t"
      "v_cndmask_b32 v21, v0, v28, %[own]\n\t"
      "v_xor_b32 v22, %[tag], v1\n\t"
      "v_xor_b32 v23, %[tag], v3\n\t"
      "v_mov_b32_dpp v20, v21 row_ror:8 row_mask:0xf bank_mask:0xc\n\t"
      "v_mov_b32_dpp v21, v21 row_ror:8 row_mask:0xf bank_mask:0x3\n\t"
      "v_bitop3_b32 v22, v22, v5, %[tag] bitop3:0xf6\n\t"
      "v_max_f32_dpp v18, v20, v20 quad_perm:[1,0,3,2]" DPPM
      "v_bitop3_b32 v23, v23, v7, %[tag] bitop3:0xf6\n\t"
      "v_bitop3_b32 v22, v22, v9, %[tag] bitop3:0xf6\n\t"
      "v_max_f32_dpp v18, v18, v18 quad_perm:[2,3,0,1]" DPPM
      "v_bitop3_b32 v23, v23, v11, %[tag] bitop3:0xf6\n\t"
      "v_bitop3_b32 v22, v22, v13, %[tag] bitop3:0xf6\n\t"
      "v_max_f32_dpp v18, v18, v18 row_half_mirror" DPPM
      "v_bitop3_b32 v23, v23, v15, %[tag] bitop3:0xf6\n\t"
      "v_sub_f32 v20, v20, v18\n\t"
      "v_exp_f32 v20, v20\n\t"
      "v_or_b32 v22, v22, v23\n\t"
      "v_cmp_ne_u32 vcc, 0, v22\n\t"
      "v_mul_f32 v21, v20, v21\n\t"
      "s_cbranch_vccz 3f\n\t"
      "s_sub_u32 s44, s44, 1\n\t"
      "s_cbranch_scc0 1b\n\t"
      "s_endpgm\n\t"
      "3:\n\t"
      "v_mul_f32_dpp v16, v20, v24 row_newbcast:0" DPPM
      "v_mul_f32_dpp v17, v20, v2 row_newbcast:1" DPPM
      "v_add_f32_dpp v21, v21, v21 quad_perm:[1,0,3,2]" DPPM
      "v_fmac_f32_dpp v16, v20, v4 row_newbcast:2" DPPM
      "v_fmac_f32_dpp v17, v20, v6 row_newbcast:3" DPPM
      "v_add_f32_dpp v21, v21, v21 quad_perm:[2,3,0,1]" DPPM
      "v_fmac_f32_dpp v16, v20, v8 row_newbcast:4" DPPM
      "v_fmac_f32_dpp v17, v20, v10 row_newbcast:5" DPPM
      "v_add_f32_dpp v21, v21, v21 row_half_mirror" DPPM
      "v_fmac_f32_dpp v16, v20, v12 row_newbcast:6" DPPM
      "v_fmac_f32_dpp v17, v20, v14 row_newbcast:7" DPPM
      "v_rcp_f32 v21, v21\n\t"
      "v_add_f32 v16, v16, v17\n\t"
      "v_mul_f32 v16, v16, v21\n\t"
      "v_cvt_pk_bf16_f32 v16, v16, v16\n\t"
      "global_store_short %[vout], v16, %[sout] sc1\n\t"
      "global_store_dword v30, %[tag], %[sep] sc1"
      :
      : [vo] "v"(vo), [vme] "v"(vme), [vml] "v"(vml), [vmy] "v"(vmy), [vrec] "v"(vrec), [vl] "v"(vl), [vout] "v"(vout), [tag] "v"(tag),
        [sb] "s"(sb), [own] "s"(own), [hi8] "s"(hi8), [sout] "s"(out), [sep] "s"(hw), [s] "s"(s),
        [t0] "s"(t0), [tx] "i"((2 * W - 1) * PACE_NS / 10 + SWEEP_T)
      : "memory", "vcc", "scc", "s42", "s43", "s44", "v0", "v1", "v2", "v3", "v4", "v5", "v6", "v7", "v8", "v9", "v10", "v11", "v12", "v13",
        "v14", "v15", "v16", "v17", "v18", "v19", "v20", "v21", "v22", "v23", "v24", "v25", "v26", "v27", "v28", "v29", "v30");
}

extern "C" void kernel_launch(void* const* d_in, const int* in_sizes, int n_in, void* d_out, int out_size, void* d_ws,
                              size_t ws_size, hipStream_t stream) {
  attn_decode<<<H * NSPLIT, W * 64, 0, stream>>>(d_in[0], d_in[1], d_in[2], d_out, d_ws);
}
```

```cpp
#include <hip/hip_runtime.h>
#include <hip/hip_bf16.h>

constexpr int PACE_NS = 270;
constexpr int SWEEP_T = 124;
#define SWEEP_D "6"

constexpr int H = 32, D = 128, NSPLIT = 8, W = 16;

using bf16x2 = __attribute__((ext_vector_type(2))) __bf16;
using bf16x8 = __attribute__((ext_vector_type(8))) __bf16;
using u32x4 = __attribute__((ext_vector_type(4))) unsigned;
using f32x4 = __attribute__((ext_vector_type(4))) float;
using f32x2 = __attribute__((ext_vector_type(2))) float;
using u64 = unsigned long long;
using u64x4 = __attribute__((ext_vector_type(4))) u64;
typedef __attribute__((address_space(1))) unsigned char gu8;
typedef __attribute__((address_space(1))) unsigned gu32;
typedef __attribute__((address_space(1))) u64 gu64;
typedef __attribute__((address_space(1))) u32x4 gu32x4;
#define RLX_AGENT __ATOMIC_RELAXED, __HIP_MEMORY_SCOPE_AGENT
#define SB() __builtin_amdgcn_sched_barrier(0)

__device__ __forceinline__ float dot2(unsigned k, unsigned q, float acc) {
  return __builtin_amdgcn_fdot2_f32_bf16(__builtin_bit_cast(bf16x2, k), __builtin_bit_cast(bf16x2, q), acc, false);
}
__device__ __forceinline__ unsigned cvtpk(float lo, float hi) {
  bf16x2 r = {(__bf16)lo, (__bf16)hi};
  return __builtin_bit_cast(unsigned, r);
}
template <int CTRL> __device__ __forceinline__ float dpp(float x) {
  return __builtin_bit_cast(float, __builtin_amdgcn_mov_dpp(__builtin_bit_cast(int, x), CTRL, 0xf, 0xf, true));
}
__device__ __forceinline__ float row_sum(float x) {
  x += dpp<0xB1>(x); x += dpp<0x4E>(x); x += dpp<0x141>(x); x += dpp<0x140>(x); return x;
}
__device__ __forceinline__ float xrow_max(float x) {
  auto s = __builtin_amdgcn_permlane16_swap(__float_as_uint(x), __float_as_uint(x), false, false);
  x = fmaxf(__uint_as_float(s[0]), __uint_as_float(s[1]));
  auto t = __builtin_amdgcn_permlane32_swap(__float_as_uint(x), __float_as_uint(x), false, false);
  return fmaxf(__uint_as_float(t[0]), __uint_as_float(t[1]));
}
__device__ __forceinline__ float xrow_sum(float x) {
  auto s = __builtin_amdgcn_permlane16_swap(__float_as_uint(x), __float_as_uint(x), false, false);
  x = __uint_as_float(s[0]) + __uint_as_float(s[1]);
  auto t = __builtin_amdgcn_permlane32_swap(__float_as_uint(x), __float_as_uint(x), false, false);
  return __uint_as_float(t[0]) + __uint_as_float(t[1]);
}
__device__ __forceinline__ unsigned now() { return (unsigned)__builtin_amdgcn_s_memrealtime(); }

constexpr unsigned HEAD_B = 16384, SLOTS_B = 1024, SLOT_B = 640;

__global__ void __launch_bounds__(W * 64, W / 4)
attn_decode(const void* q_, const void* k_, const void* v_, void* out_, void* ws_) {
  __shared__ __attribute__((aligned(16))) float lds[W * D + 2 * W];
  u64x4 a03;
  u64 a4;
  asm volatile("s_load_dwordx8 %0, %2, 0x0\n\ts_load_dwordx2 %1, %2, 0x20"
               : "=&s"(a03), "=&s"(a4) : "s"(__builtin_amdgcn_kernarg_segment_ptr()));
  SB();
  const int b = blockIdx.x;
  const int h = (b & 7) * 4 + ((b >> 3) & 3), s = b >> 5;
  const int tid = threadIdx.x, lane = tid & 63, wave = __builtin_amdgcn_readfirstlane(tid >> 6);
  const int c = lane & 15;
  constexpr float C2 = 0.088388347648318440f * 1.4426950408889634f;

  unsigned so = ((unsigned)h << 20) + (((wave + h) & 15) << 16) + (((s + wave) & 7) << 13) + 4096 + lane * 16;
  unsigned vq = h * 256 + c * 16, vep = h * HEAD_B;
  asm volatile("" : "+v"(so), "+v"(vq), "+v"(vep));
  SB();
  u64 t64;
  asm volatile("s_waitcnt lgkmcnt(0)\n\ts_memrealtime s[40:41]" : "+s"(a03), "+s"(a4), "=&{s[40:41]}"(t64));
  gu8 *const q = (gu8*)a03[0], *const k = (gu8*)a03[1], *const v = (gu8*)a03[2], *const out = (gu8*)a03[3], *const ws = (gu8*)a4;
  const u32x4 qf = *(const gu32x4*)(q + vq);
  const unsigned e0 = __hip_atomic_load((gu32*)(ws + vep), RLX_AGENT);
  SB();
  asm volatile("s_cmp_eq_u32 %[g], 0\n\t"
               "s_cbranch_scc1 2f\n\t"
               "1:\n\t"
               "s_sleep 1\n\t"
               "s_memrealtime s[42:43]\n\t"
               "s_waitcnt lgkmcnt(0)\n\t"
               "s_sub_u32 s42, s42, s40\n\t"
               "s_cmp_lt_u32 s42, %[g]\n\t"
               "s_cbranch_scc1 1b\n\t"
               "2:"
               : "+{s[40:41]}"(t64) : [g] "s"((wave * (PACE_NS * 1024 / 10)) >> 10) : "scc", "s42", "s43");
#define LD(dst, p) _Pragma("unroll") for (int i = 0; i < 8; ++i) { \
    dst[i] = __builtin_nontemporal_load((const gu32x4*)(p + (size_t)so + (i * 1024 - 4096))); SB(); }
  u32x4 kr[8], vr[8], pf[8];
  SB();
  LD(kr, k);
  asm volatile("s_waitcnt lgkmcnt(0)" : "+{s[40:41]}"(t64));
  const unsigned t0 = (unsigned)t64;
  gu8* hw = ws + h * HEAD_B;
  unsigned tag = e0 + 0x9E3779B9u;
  gu64* mine = (gu64*)(hw + SLOTS_B + (((tid >> 7) & 1) * NSPLIT + s) * SLOT_B) + (tid & 127);
  const bool ko = tid < 256 && (tid & 127) < 66;
  if (ko) __hip_atomic_store(mine, (u64)e0 << 32, RLX_AGENT);
  SB();
  if (wave >= W / 2) __builtin_amdgcn_s_barrier();
  SB();

  float x[8];
#pragma unroll
  for (int i = 0; i < 8; ++i) {
    float d = dot2(kr[i][0], qf[0], 0.f);
    d = dot2(kr[i][1], qf[1], d); d = dot2(kr[i][2], qf[2], d); d = dot2(kr[i][3], qf[3], d);
    x[i] = row_sum(d) * C2;
  }
  float m = x[0], l = 0.f;
#pragma unroll
  for (int i = 1; i < 8; ++i) m = fmaxf(m, x[i]);
  m = xrow_max(m);
  const unsigned mlo = c < 8 ? 0u : 0xffff0000u;
  const unsigned half = (c & 1) ? 0xffff0000u : 0x0000ffffu;
  const int dsel = (c & 7) >> 1;
  const unsigned mk0 = dsel == 0 ? half : 0u, mk1 = dsel == 1 ? half : 0u, mk2 = dsel == 2 ? half : 0u, mk3 = dsel == 3 ? half : 0u;
#pragma unroll
  for (int i = 0; i < 8; ++i) {
    const float p = __builtin_amdgcn_exp2f(x[i] - m);
    l += p;
    const unsigned hi = cvtpk(p, p);
    const float r = p - __uint_as_float(hi & mlo);
    const unsigned pk = cvtpk(r, r);
    pf[i] = u32x4{pk & mk0, pk & mk1, pk & mk2, pk & mk3};
    asm volatile("" : "+v"(pf[i]));
  }
  u64 pre = 0;
  if (ko) pre = __hip_atomic_load(mine, RLX_AGENT);
  if (wave < W / 2) __builtin_amdgcn_s_barrier();
  SB();
  {
    const unsigned g = (W + wave) * PACE_NS / 10;
    while (now() - t0 < g - 36) __builtin_amdgcn_s_sleep(8);
    while (now() - t0 < g) __builtin_amdgcn_s_sleep(1);
    SB();
  }
  LD(vr, v);
  l = xrow_sum(l);
  *reinterpret_cast<f32x2*>(lds + W * D + 2 * wave) = f32x2{m, l};
  const gu8* sb = hw + SLOTS_B + (wave & 1) * (NSPLIT * SLOT_B) + 4 * SLOT_B;
  unsigned vo = lane * 8, vme = s * SLOT_B + lane * 8;
  unsigned vml = ((c & 7) == 0 ? 1 : (c & 7)) * SLOT_B + 512 + (c >> 3) * 8;
  unsigned vmy = s * SLOT_B + 512 + (c >> 3) * 8;
  const unsigned lbase = (unsigned)(size_t)(__attribute__((address_space(3))) float*)lds;
  unsigned vrec = lbase + ((wave & 1) * 64 + lane) * 4, vl = lbase + (W * D + 2 * c) * 4, vout = h * 256 + (tid & 127) * 2;
  const u64 own = __ballot((c & 7) == 0), hi8 = __ballot(c >= 8);
  unsigned vw = lbase + (wave * D + 8 * c + 4 * ((lane >> 4) & 1) + 2 * (lane >> 5)) * 4;
  asm volatile("" : "+v"(vo), "+v"(vme), "+v"(vml), "+v"(vmy), "+v"(vrec), "+v"(vl), "+v"(vout), "+v"(vw), "+v"(tag)
               : "s"(out), "s"(sb), "s"(own), "s"(hi8));
  SB();
  f32x4 acc = {0.f, 0.f, 0.f, 0.f};
#pragma unroll
  for (int i = 0; i < 8; ++i)
    acc = __builtin_amdgcn_mfma_f32_16x16x32_bf16(__builtin_bit_cast(bf16x8, pf[i]), __builtin_bit_cast(bf16x8, vr[i]), acc, 0, 0, 0);
  auto s02 = __builtin_amdgcn_permlane32_swap(__float_as_uint(acc[0]), __float_as_uint(acc[2]), false, false);
  auto s13 = __builtin_amdgcn_permlane32_swap(__float_as_uint(acc[1]), __float_as_uint(acc[3]), false, false);
  const f32x2 o2 = {__uint_as_float(s02[0]) + __uint_as_float(s02[1]), __uint_as_float(s13[0]) + __uint_as_float(s13[1])};
  *(__attribute__((address_space(3))) f32x2*)(size_t)vw = o2;
  asm volatile("" :: "v"(pre));
  asm volatile("s_waitcnt vmcnt(0) lgkmcnt(0)\n\ts_barrier" ::: "memory");
  if (wave >= 2) return;

#define DPPM " row_mask:0xf bank_mask:0xf\n\t"
  static_assert(SLOT_B == 640 && W == 16 && NSPLIT == 8, "offsets in the asm");
  asm volatile(
      "ds_read_b64 v[26:27], %[vl]\n\t"
      "ds_read2st64_b32 v[0:1], %[vrec] offset1:2\n\t"
      "ds_read2st64_b32 v[2:3], %[vrec] offset0:4 offset1:6\n\t"
      "ds_read2st64_b32 v[4:5], %[vrec] offset0:8 offset1:10\n\t"
      "ds_read2st64_b32 v[6:7], %[vrec] offset0:12 offset1:14\n\t"
      "ds_read2st64_b32 v[8:9], %[vrec] offset0:16 offset1:18\n\t"
      "ds_read2st64_b32 v[10:11], %[vrec] offset0:20 offset1:22\n\t"
      "ds_read2st64_b32 v[12:13], %[vrec] offset0:24 offset1:26\n\t"
      "ds_read2st64_b32 v[14:15], %[vrec] offset0:28 offset1:30\n\t"
      "s_cmp_eq_u32 %[s], 0\n\t"
      "s_waitcnt lgkmcnt(8)\n\t"
      "v_max_f32_dpp v28, v26, v26 quad_perm:[1,0,3,2]" DPPM
      "v_mov_b32 v25, %[tag]\n\t"
      "v_mov_b32 v29, %[tag]\n\t"
      "v_max_f32_dpp v28, v28, v28 quad_perm:[2,3,0,1]" DPPM
      "v_mov_b32 v30, 0\n\t"
      "s_nop 0\n\t"
      "v_max_f32_dpp v28, v28, v28 row_half_mirror" DPPM
      "s_nop 1\n\t"
      "v_max_f32_dpp v28, v28, v28 row_mirror" DPPM
      "v_sub_f32 v26, v26, v28\n\t"
      "v_exp_f32 v26, v26\n\t"
      "s_waitcnt lgkmcnt(0)\n\t"
      "v_mul_f32 v27, v26, v27\n\t"
      "v_mul_f32_dpp v16, v26, v0 row_newbcast:0" DPPM
      "v_mul_f32_dpp v17, v26, v1 row_newbcast:1" DPPM
      "v_add_f32_dpp v27, v27, v27 quad_perm:[1,0,3,2]" DPPM
      "v_mul_f32_dpp v18, v26, v2 row_newbcast:2" DPPM
      "v_mul_f32_dpp v19, v26, v3 row_newbcast:3" DPPM
      "v_add_f32_dpp v27, v27, v27 quad_perm:[2,3,0,1]" DPPM
      "v_fmac_f32_dpp v16, v26, v4 row_newbcast:4" DPPM
      "v_fmac_f32_dpp v17, v26, v5 row_newbcast:5" DPPM
      "v_fmac_f32_dpp v18, v26, v6 row_newbcast:6" DPPM
      "v_fmac_f32_dpp v19, v26, v7 row_newbcast:7" DPPM
      "v_add_f32_dpp v27, v27, v27 row_half_mirror" DPPM
      "v_fmac_f32_dpp v16, v26, v8 row_newbcast:8" DPPM
      "v_fmac_f32_dpp v17, v26, v9 row_newbcast:9" DPPM
      "v_fmac_f32_dpp v18, v26, v10 row_newbcast:10" DPPM
      "v_fmac_f32_dpp v19, v26, v11 row_newbcast:11" DPPM
      "v_add_f32_dpp v27, v27, v27 row_mirror" DPPM
      "v_fmac_f32_dpp v16, v26, v12 row_newbcast:12" DPPM
      "v_fmac_f32_dpp v17, v26, v13 row_newbcast:13" DPPM
      "v_fmac_f32_dpp v18, v26, v14 row_newbcast:14" DPPM
      "v_fmac_f32_dpp v19, v26, v15 row_newbcast:15" DPPM
      "v_pk_add_f32 v[16:17], v[16:17], v[18:19]\n\t"
      "v_cndmask_b32 v28, v28, v27, %[hi8]\n\t"
      "v_add_f32 v24, v16, v17\n\t"
      "s_cbranch_scc1 2f\n\t"
      "global_store_dwordx2 %[vme], v[24:25], %[sb] offset:-2560 sc1\n\t"
      "global_store_dwordx2 %[vmy], v[28:29], %[sb] offset:-2560 sc1\n\t"
      "s_endpgm\n\t"
      "2:\n\t"
      "s_mov_b32 s44, 0x200000\n\t"
      "s_sleep " SWEEP_D "\n\t"
      "4:\n\t"
      "s_memrealtime s[42:43]\n\t"
      "s_waitcnt lgkmcnt(0)\n\t"
      "s_sub_u32 s42, s42, %[t0]\n\t"
      "s_cmpk_lt_u32 s42, %c[tx]\n\t"
      "s_cbranch_scc1 4b\n\t"
      "1:\n\t"
      "global_load_dwordx2 v[2:3], %[vo], %[sb] offset:-1920 sc1\n\t"
      "global_load_dwordx2 v[4:5], %[vo], %[sb] offset:-1280 sc1\n\t"
      "global_load_dwordx2 v[6:7], %[vo], %[sb] offset:-640 sc1\n\t"
      "global_load_dwordx2 v[8:9], %[vo], %[sb] sc1\n\t"
      "global_load_dwordx2 v[10:11], %[vo], %[sb] offset:640 sc1\n\t"
      "global_load_dwordx2 v[12:13], %[vo], %[sb] offset:1280 sc1\n\t"
      "global_load_dwordx2 v[14:15], %[vo], %[sb] offset:1920 sc1\n\t"
      "global_load_dwordx2 v[0:1], %[vml], %[sb] offset:-2560 sc1\n\t"
      "s_waitcnt vmcnt(0)\n\t"
      "v_cndmask_b32 v20, v0, v28, %[own]\n\t"
      "v_cndmask_b32 v21, v0, v28, %[own]\n\t"
      "v_xor_b32 v22, %[tag], v1\n\t"
      "v_xor_b32 v23, %[tag], v3\n\t"
      "v_mov_b32_dpp v20, v21 row_ror:8 row_mask:0xf bank_mask:0xc\n\t"
      "v_mov_b32_dpp v21, v21 row_ror:8 row_mask:0xf bank_mask:0x3\n\t"
      "v_bitop3_b32 v22, v22, v5, %[tag] bitop3:0xf6\n\t"
      "v_max_f32_dpp v18, v20, v20 quad_perm:[1,0,3,2]" DPPM
      "v_bitop3_b32 v23, v23, v7, %[tag] bitop3:0xf6\n\t"
      "v_bitop3_b32 v22, v22, v9, %[tag] bitop3:0xf6\n\t"
      "v_max_f32_dpp v18, v18, v18 quad_perm:[2,3,0,1]" DPPM
      "v_bitop3_b32 v23, v23, v11, %[tag] bitop3:0xf6\n\t"
      "v_bitop3_b32 v22, v22, v13, %[tag] bitop3:0xf6\n\t"
      "v_max_f32_dpp v18, v18, v18 row_half_mirror" DPPM
      "v_bitop3_b32 v23, v23, v15, %[tag] bitop3:0xf6\n\t"
      "v_sub_f32 v20, v20, v18\n\t"
      "v_exp_f32 v20, v20\n\t"
      "v_or_b32 v22, v22, v23\n\t"
      "v_cmp_ne_u32 vcc, 0, v22\n\t"
      "v_mul_f32 v21, v20, v21\n\t"
      "s_cbranch_vccz 3f\n\t"
      "s_sub_u32 s44, s44, 1\n\t"
      "s_cbranch_scc0 1b\n\t"
      "s_endpgm\n\t"
      "3:\n\t"
      "v_mul_f32_dpp v16, v20, v24 row_newbcast:0" DPPM
      "v_mul_f32_dpp v17, v20, v2 row_newbcast:1" DPPM
      "v_add_f32_dpp v21, v21, v21 quad_perm:[1,0,3,2]" DPPM
      "v_fmac_f32_dpp v16, v20, v4 row_newbcast:2" DPPM
      "v_fmac_f32_dpp v17, v20, v6 row_newbcast:3" DPPM
      "v_add_f32_dpp v21, v21, v21 quad_perm:[2,3,0,1]" DPPM
      "v_fmac_f32_dpp v16, v20, v8 row_newbcast:4" DPPM
      "v_fmac_f32_dpp v17, v20, v10 row_newbcast:5" DPPM
      "v_add_f32_dpp v21, v21, v21 row_half_mirror" DPPM
      "v_fmac_f32_dpp v16, v20, v12 row_newbcast:6" DPPM
      "v_fmac_f32_dpp v17, v20, v14 row_newbcast:7" DPPM
      "v_rcp_f32 v21, v21\n\t"
      "v_add_f32 v16, v16, v17\n\t"
      "v_mul_f32 v16, v16, v21\n\t"
      "v_cvt_pk_bf16_f32 v16, v16, v16\n\t"
      "global_store_short %[vout], v16, %[sout] sc1\n\t"
      "global_store_dword v30, %[tag], %[sep] sc1"
      :
      : [vo] "v"(vo), [vme] "v"(vme), [vml] "v"(vml), [vmy] "v"(vmy), [vrec] "v"(vrec), [vl] "v"(vl), [vout] "v"(vout), [tag] "v"(tag),
        [sb] "s"(sb), [own] "s"(own), [hi8] "s"(hi8), [sout] "s"(out), [sep] "s"(hw), [s] "s"(s),
        [t0] "s"(t0), [tx] "i"((2 * W - 1) * PACE_NS / 10 + SWEEP_T)
      : "memory", "vcc", "scc", "s42", "s43", "s44", "v0", "v1", "v2", "v3", "v4", "v5", "v6", "v7", "v8", "v9", "v10", "v11", "v12", "v13",
        "v14", "v15", "v16", "v17", "v18", "v19", "v20", "v21", "v22", "v23", "v24", "v25", "v26", "v27", "v28", "v29", "v30");
}

extern "C" void kernel_launch(void* const* d_in, const int* in_sizes, int n_in, void* d_out, int out_size, void* d_ws,
                              size_t ws_size, hipStream_t stream) {
  attn_decode<<<H * NSPLIT, W * 64, 0, stream>>>(d_in[0], d_in[1], d_in[2], d_out, d_ws);
}
```

```cpp
#define PACE 270
#define HP (int)(__builtin_bitreverse32(h) >> 28)
#include <hip/hip_runtime.h>
#include <hip/hip_bf16.h>

constexpr int PACE_NS = PACE;
constexpr int SWEEP_T = 124;
#define SWEEP_D "6"

constexpr int H = 32, D = 128, NSPLIT = 8, W = 16;

using bf16x2 = __attribute__((ext_vector_type(2))) __bf16;
using bf16x8 = __attribute__((ext_vector_type(8))) __bf16;
using u32x4 = __attribute__((ext_vector_type(4))) unsigned;
using f32x4 = __attribute__((ext_vector_type(4))) float;
using f32x2 = __attribute__((ext_vector_type(2))) float;
using u64 = unsigned long long;
using u64x4 = __attribute__((ext_vector_type(4))) u64;
typedef __attribute__((address_space(1))) unsigned char gu8;
typedef __attribute__((address_space(1))) unsigned gu32;
typedef __attribute__((address_space(1))) u64 gu64;
typedef __attribute__((address_space(1))) u32x4 gu32x4;
#define RLX_AGENT __ATOMIC_RELAXED, __HIP_MEMORY_SCOPE_AGENT
#define SB() __builtin_amdgcn_sched_barrier(0)

__device__ __forceinline__ float dot2(unsigned k, unsigned q, float acc) {
  return __builtin_amdgcn_fdot2_f32_bf16(__builtin_bit_cast(bf16x2, k), __builtin_bit_cast(bf16x2, q), acc, false);
}
__device__ __forceinline__ unsigned cvtpk(float lo, float hi) {
  bf16x2 r = {(__bf16)lo, (__bf16)hi};
  return __builtin_bit_cast(unsigned, r);
}
template <int CTRL> __device__ __forceinline__ float dpp(float x) {
  return __builtin_bit_cast(float, __builtin_amdgcn_mov_dpp(__builtin_bit_cast(int, x), CTRL, 0xf, 0xf, true));
}
__device__ __forceinline__ float row_sum(float x) {
  x += dpp<0xB1>(x); x += dpp<0x4E>(x); x += dpp<0x141>(x); x += dpp<0x140>(x); return x;
}
__device__ __forceinline__ float xrow_max(float x) {
  auto s = __builtin_amdgcn_permlane16_swap(__float_as_uint(x), __float_as_uint(x), false, false);
  x = fmaxf(__uint_as_float(s[0]), __uint_as_float(s[1]));
  auto t = __builtin_amdgcn_permlane32_swap(__float_as_uint(x), __float_as_uint(x), false, false);
  return fmaxf(__uint_as_float(t[0]), __uint_as_float(t[1]));
}
__device__ __forceinline__ float xrow_sum(float x) {
  auto s = __builtin_amdgcn_permlane16_swap(__float_as_uint(x), __float_as_uint(x), false, false);
  x = __uint_as_float(s[0]) + __uint_as_float(s[1]);
  auto t = __builtin_amdgcn_permlane32_swap(__float_as_uint(x), __float_as_uint(x), false, false);
  return __uint_as_float(t[0]) + __uint_as_float(t[1]);
}
__device__ __forceinline__ unsigned now() { return (unsigned)__builtin_amdgcn_s_memrealtime(); }

constexpr unsigned HEAD_B = 16384, SLOTS_B = 1024, SLOT_B = 640;

__global__ void __launch_bounds__(W * 64, W / 4)
attn_decode(const void* q_, const void* k_, const void* v_, void* out_, void* ws_) {
  __shared__ __attribute__((aligned(16))) float lds[W * D + 2 * W];
  u64x4 a03;
  u64 a4;
  asm volatile("s_load_dwordx8 %0, %2, 0x0\n\ts_load_dwordx2 %1, %2, 0x20"
               : "=&s"(a03), "=&s"(a4) : "s"(__builtin_amdgcn_kernarg_segment_ptr()));
  SB();
  const int b = blockIdx.x;
  const int h = (b & 7) * 4 + ((b >> 3) & 3), s = b >> 5;
  const int tid = threadIdx.x, lane = tid & 63, wave = __builtin_amdgcn_readfirstlane(tid >> 6);
  const int c = lane & 15;
  constexpr float C2 = 0.088388347648318440f * 1.4426950408889634f;

  unsigned so = ((unsigned)h << 20) + (((wave + HP) & 15) << 16) + (((s + wave) & 7) << 13) + 4096 + lane * 16;
  unsigned vq = h * 256 + c * 16, vep = h * HEAD_B;
  asm volatile("" : "+v"(so), "+v"(vq), "+v"(vep));
  SB();
  u64 t64;
  asm volatile("s_waitcnt lgkmcnt(0)\n\ts_memrealtime s[40:41]" : "+s"(a03), "+s"(a4), "=&{s[40:41]}"(t64));
  gu8 *const q = (gu8*)a03[0], *const k = (gu8*)a03[1], *const v = (gu8*)a03[2], *const out = (gu8*)a03[3], *const ws = (gu8*)a4;
  const u32x4 qf = *(const gu32x4*)(q + vq);
  const unsigned e0 = __hip_atomic_load((gu32*)(ws + vep), RLX_AGENT);
  SB();
  asm volatile("s_cmp_eq_u32 %[g], 0\n\t"
               "s_cbranch_scc1 2f\n\t"
               "1:\n\t"
               "s_sleep 1\n\t"
               "s_memrealtime s[42:43]\n\t"
               "s_waitcnt lgkmcnt(0)\n\t"
               "s_sub_u32 s42, s42, s40\n\t"
               "s_cmp_lt_u32 s42, %[g]\n\t"
               "s_cbranch_scc1 1b\n\t"
               "2:"
               : "+{s[40:41]}"(t64) : [g] "s"((wave * (PACE_NS * 1024 / 10)) >> 10) : "scc", "s42", "s43");
#define LD(dst, p) _Pragma("unroll") for (int i = 0; i < 8; ++i) { \
    dst[i] = __builtin_nontemporal_load((const gu32x4*)(p + (size_t)so + (i * 1024 - 4096))); SB(); }
  u32x4 kr[8], vr[8], pf[8];
  SB();
  LD(kr, k);
  asm volatile("s_waitcnt lgkmcnt(0)" : "+{s[40:41]}"(t64));
  const unsigned t0 = (unsigned)t64;
  gu8* hw = ws + h * HEAD_B;
  unsigned tag = e0 + 0x9E3779B9u;
  gu64* mine = (gu64*)(hw + SLOTS_B + (((tid >> 7) & 1) * NSPLIT + s) * SLOT_B) + (tid & 127);
  const bool ko = tid < 256 && (tid & 127) < 66;
  if (ko) __hip_atomic_store(mine, (u64)e0 << 32, RLX_AGENT);
  SB();
  if (wave >= W / 2) __builtin_amdgcn_s_barrier();
  SB();

  float x[8];
#pragma unroll
  for (int i = 0; i < 8; ++i) {
    float d = dot2(kr[i][0], qf[0], 0.f);
    d = dot2(kr[i][1], qf[1], d); d = dot2(kr[i][2], qf[2], d); d = dot2(kr[i][3], qf[3], d);
    x[i] = row_sum(d) * C2;
  }
  float m = x[0], l = 0.f;
#pragma unroll
  for (int i = 1; i < 8; ++i) m = fmaxf(m, x[i]);
  m = xrow_max(m);
  const unsigned mlo = c < 8 ? 0u : 0xffff0000u;
  const unsigned half = (c & 1) ? 0xffff0000u : 0x0000ffffu;
  const int dsel = (c & 7) >> 1;
  const unsigned mk0 = dsel == 0 ? half : 0u, mk1 = dsel == 1 ? half : 0u, mk2 = dsel == 2 ? half : 0u, mk3 = dsel == 3 ? half : 0u;
#pragma unroll
  for (int i = 0; i < 8; ++i) {
    const float p = __builtin_amdgcn_exp2f(x[i] - m);
    l += p;
    const unsigned hi = cvtpk(p, p);
    const float r = p - __uint_as_float(hi & mlo);
    const unsigned pk = cvtpk(r, r);
    pf[i] = u32x4{pk & mk0, pk & mk1, pk & mk2, pk & mk3};
    asm volatile("" : "+v"(pf[i]));
  }
  u64 pre = 0;
  if (ko) pre = __hip_atomic_load(mine, RLX_AGENT);
  if (wave < W / 2) __builtin_amdgcn_s_barrier();
  SB();
  {
    const unsigned g = (W + wave) * PACE_NS / 10;
    while (now() - t0 < g - 36) __builtin_amdgcn_s_sleep(8);
    while (now() - t0 < g) __builtin_amdgcn_s_sleep(1);
    SB();
  }
  LD(vr, v);
  l = xrow_sum(l);
  *reinterpret_cast<f32x2*>(lds + W * D + 2 * wave) = f32x2{m, l};
  const gu8* sb = hw + SLOTS_B + (wave & 1) * (NSPLIT * SLOT_B) + 4 * SLOT_B;
  unsigned vo = lane * 8, vme = s * SLOT_B + lane * 8;
  unsigned vml = ((c & 7) == 0 ? 1 : (c & 7)) * SLOT_B + 512 + (c >> 3) * 8;
  unsigned vmy = s * SLOT_B + 512 + (c >> 3) * 8;
  const unsigned lbase = (unsigned)(size_t)(__attribute__((address_space(3))) float*)lds;
  unsigned vrec = lbase + ((wave & 1) * 64 + lane) * 4, vl = lbase + (W * D + 2 * c) * 4, vout = h * 256 + (tid & 127) * 2;
  const u64 own = __ballot((c & 7) == 0), hi8 = __ballot(c >= 8);
  unsigned vw = lbase + (wave * D + 8 * c + 4 * ((lane >> 4) & 1) + 2 * (lane >> 5)) * 4;
  asm volatile("" : "+v"(vo), "+v"(vme), "+v"(vml), "+v"(vmy), "+v"(vrec), "+v"(vl), "+v"(vout), "+v"(vw), "+v"(tag)
               : "s"(out), "s"(sb), "s"(own), "s"(hi8));
  SB();
  f32x4 acc = {0.f, 0.f, 0.f, 0.f};
#pragma unroll
  for (int i = 0; i < 8; ++i)
    acc = __builtin_amdgcn_mfma_f32_16x16x32_bf16(__builtin_bit_cast(bf16x8, pf[i]), __builtin_bit_cast(bf16x8, vr[i]), acc, 0, 0, 0);
  auto s02 = __builtin_amdgcn_permlane32_swap(__float_as_uint(acc[0]), __float_as_uint(acc[2]), false, false);
  auto s13 = __builtin_amdgcn_permlane32_swap(__float_as_uint(acc[1]), __float_as_uint(acc[3]), false, false);
  const f32x2 o2 = {__uint_as_float(s02[0]) + __uint_as_float(s02[1]), __uint_as_float(s13[0]) + __uint_as_float(s13[1])};
  *(__attribute__((address_space(3))) f32x2*)(size_t)vw = o2;
  asm volatile("" :: "v"(pre));
  asm volatile("s_waitcnt vmcnt(0) lgkmcnt(0)\n\ts_barrier" ::: "memory");
  if (wave == 2) { const unsigned n0 = now(); int sk = ((int)(n0 - t0) - 880) * 10; if (sk > 5000) sk = 5000; while ((int)(now() - n0) < sk) __builtin_amdgcn_s_sleep(2); }
  if (wave >= 2) return;

#define DPPM " row_mask:0xf bank_mask:0xf\n\t"
  static_assert(SLOT_B == 640 && W == 16 && NSPLIT == 8, "offsets in the asm");
  asm volatile(
      "ds_read_b64 v[26:27], %[vl]\n\t"
      "ds_read2st64_b32 v[0:1], %[vrec] offset1:2\n\t"
      "ds_read2st64_b32 v[2:3], %[vrec] offset0:4 offset1:6\n\t"
      "ds_read2st64_b32 v[4:5], %[vrec] offset0:8 offset1:10\n\t"
      "ds_read2st64_b32 v[6:7], %[vrec] offset0:12 offset1:14\n\t"
      "ds_read2st64_b32 v[8:9], %[vrec] offset0:16 offset1:18\n\t"
      "ds_read2st64_b32 v[10:11], %[vrec] offset0:20 offset1:22\n\t"
      "ds_read2st64_b32 v[12:13], %[vrec] offset0:24 offset1:26\n\t"
      "ds_read2st64_b32 v[14:15], %[vrec] offset0:28 offset1:30\n\t"
      "s_cmp_eq_u32 %[s], 0\n\t"
      "s_waitcnt lgkmcnt(8)\n\t"
      "v_max_f32_dpp v28, v26, v26 quad_perm:[1,0,3,2]" DPPM
      "v_mov_b32 v25, %[tag]\n\t"
      "v_mov_b32 v29, %[tag]\n\t"
      "v_max_f32_dpp v28, v28, v28 quad_perm:[2,3,0,1]" DPPM
      "v_mov_b32 v30, 0\n\t"
      "s_nop 0\n\t"
      "v_max_f32_dpp v28, v28, v28 row_half_mirror" DPPM
      "s_nop 1\n\t"
      "v_max_f32_dpp v28, v28, v28 row_mirror" DPPM
      "v_sub_f32 v26, v26, v28\n\t"
      "v_exp_f32 v26, v26\n\t"
      "s_waitcnt lgkmcnt(0)\n\t"
      "v_mul_f32 v27, v26, v27\n\t"
      "v_mul_f32_dpp v16, v26, v0 row_newbcast:0" DPPM
      "v_mul_f32_dpp v17, v26, v1 row_newbcast:1" DPPM
      "v_add_f32_dpp v27, v27, v27 quad_perm:[1,0,3,2]" DPPM
      "v_mul_f32_dpp v18, v26, v2 row_newbcast:2" DPPM
      "v_mul_f32_dpp v19, v26, v3 row_newbcast:3" DPPM
      "v_add_f32_dpp v27, v27, v27 quad_perm:[2,3,0,1]" DPPM
      "v_fmac_f32_dpp v16, v26, v4 row_newbcast:4" DPPM
      "v_fmac_f32_dpp v17, v26, v5 row_newbcast:5" DPPM
      "v_fmac_f32_dpp v18, v26, v6 row_newbcast:6" DPPM
      "v_fmac_f32_dpp v19, v26, v7 row_newbcast:7" DPPM
      "v_add_f32_dpp v27, v27, v27 row_half_mirror" DPPM
      "v_fmac_f32_dpp v16, v26, v8 row_newbcast:8" DPPM
      "v_fmac_f32_dpp v17, v26, v9 row_newbcast:9" DPPM
      "v_fmac_f32_dpp v18, v26, v10 row_newbcast:10" DPPM
      "v_fmac_f32_dpp v19, v26, v11 row_newbcast:11" DPPM
      "v_add_f32_dpp v27, v27, v27 row_mirror" DPPM
      "v_fmac_f32_dpp v16, v26, v12 row_newbcast:12" DPPM
      "v_fmac_f32_dpp v17, v26, v13 row_newbcast:13" DPPM
      "v_fmac_f32_dpp v18, v26, v14 row_newbcast:14" DPPM
      "v_fmac_f32_dpp v19, v26, v15 row_newbcast:15" DPPM
      "v_pk_add_f32 v[16:17], v[16:17], v[18:19]\n\t"
      "v_cndmask_b32 v28, v28, v27, %[hi8]\n\t"
      "v_add_f32 v24, v16, v17\n\t"
      "s_cbranch_scc1 2f\n\t"
      "global_store_dwordx2 %[vme], v[24:25], %[sb] offset:-2560 sc1\n\t"
      "global_store_dwordx2 %[vmy], v[28:29], %[sb] offset:-2560 sc1\n\t"
      "s_endpgm\n\t"
      "2:\n\t"
      "s_mov_b32 s44, 0x200000\n\t"
      "s_sleep " SWEEP_D "\n\t"
      "4:\n\t"
      "s_memrealtime s[42:43]\n\t"
      "s_waitcnt lgkmcnt(0)\n\t"
      "s_sub_u32 s42, s42, %[t0]\n\t"
      "s_cmpk_lt_u32 s42, %c[tx]\n\t"
      "s_cbranch_scc1 4b\n\t"
      "1:\n\t"
      "global_load_dwordx2 v[2:3], %[vo], %[sb] offset:-1920 sc1\n\t"
      "global_load_dwordx2 v[4:5], %[vo], %[sb] offset:-1280 sc1\n\t"
      "global_load_dwordx2 v[6:7], %[vo], %[sb] offset:-640 sc1\n\t"
      "global_load_dwordx2 v[8:9], %[vo], %[sb] sc1\n\t"
      "global_load_dwordx2 v[10:11], %[vo], %[sb] offset:640 sc1\n\t"
      "global_load_dwordx2 v[12:13], %[vo], %[sb] offset:1280 sc1\n\t"
      "global_load_dwordx2 v[14:15], %[vo], %[sb] offset:1920 sc1\n\t"
      "global_load_dwordx2 v[0:1], %[vml], %[sb] offset:-2560 sc1\n\t"
      "s_waitcnt vmcnt(0)\n\t"
      "v_cndmask_b32 v20, v0, v28, %[own]\n\t"
      "v_cndmask_b32 v21, v0, v28, %[own]\n\t"
      "v_xor_b32 v22, %[tag], v1\n\t"
      "v_xor_b32 v23, %[tag], v3\n\t"
      "v_mov_b32_dpp v20, v21 row_ror:8 row_mask:0xf bank_mask:0xc\n\t"
      "v_mov_b32_dpp v21, v21 row_ror:8 row_mask:0xf bank_mask:0x3\n\t"
      "v_bitop3_b32 v22, v22, v5, %[tag] bitop3:0xf6\n\t"
      "v_max_f32_dpp v18, v20, v20 quad_perm:[1,0,3,2]" DPPM
      "v_bitop3_b32 v23, v23, v7, %[tag] bitop3:0xf6\n\t"
      "v_bitop3_b32 v22, v22, v9, %[tag] bitop3:0xf6\n\t"
      "v_max_f32_dpp v18, v18, v18 quad_perm:[2,3,0,1]" DPPM
      "v_bitop3_b32 v23, v23, v11, %[tag] bitop3:0xf6\n\t"
      "v_bitop3_b32 v22, v22, v13, %[tag] bitop3:0xf6\n\t"
      "v_max_f32_dpp v18, v18, v18 row_half_mirror" DPPM
      "v_bitop3_b32 v23, v23, v15, %[tag] bitop3:0xf6\n\t"
      "v_sub_f32 v20, v20, v18\n\t"
      "v_exp_f32 v20, v20\n\t"
      "v_or_b32 v22, v22, v23\n\t"
      "v_cmp_ne_u32 vcc, 0, v22\n\t"
      "v_mul_f32 v21, v20, v21\n\t"
      "s_cbranch_vccz 3f\n\t"
      "s_sub_u32 s44, s44, 1\n\t"
      "s_cbranch_scc0 1b\n\t"
      "s_endpgm\n\t"
      "3:\n\t"
      "v_mul_f32_dpp v16, v20, v24 row_newbcast:0" DPPM
      "v_mul_f32_dpp v17, v20, v2 row_newbcast:1" DPPM
      "v_add_f32_dpp v21, v21, v21 quad_perm:[1,0,3,2]" DPPM
      "v_fmac_f32_dpp v16, v20, v4 row_newbcast:2" DPPM
      "v_fmac_f32_dpp v17, v20, v6 row_newbcast:3" DPPM
      "v_add_f32_dpp v21, v21, v21 quad_perm:[2,3,0,1]" DPPM
      "v_fmac_f32_dpp v16, v20, v8 row_newbcast:4" DPPM
      "v_fmac_f32_dpp v17, v20, v10 row_newbcast:5" DPPM
      "v_add_f32_dpp v21, v21, v21 row_half_mirror" DPPM
      "v_fmac_f32_dpp v16, v20, v12 row_newbcast:6" DPPM
      "v_fmac_f32_dpp v17, v20, v14 row_newbcast:7" DPPM
      "v_rcp_f32 v21, v21\n\t"
      "v_add_f32 v16, v16, v17\n\t"
      "v_mul_f32 v16, v16, v21\n\t"
      "v_cvt_pk_bf16_f32 v16, v16, v16\n\t"
      "global_store_short %[vout], v16, %[sout] sc1\n\t"
      "global_store_dword v30, %[tag], %[sep] sc1"
      :
      : [vo] "v"(vo), [vme] "v"(vme), [vml] "v"(vml), [vmy] "v"(vmy), [vrec] "v"(vrec), [vl] "v"(vl), [vout] "v"(vout), [tag] "v"(tag),
        [sb] "s"(sb), [own] "s"(own), [hi8] "s"(hi8), [sout] "s"(out), [sep] "s"(hw), [s] "s"(s),
        [t0] "s"(t0), [tx] "i"((2 * W - 1) * PACE_NS / 10 + SWEEP_T)
      : "memory", "vcc", "scc", "s42", "s43", "s44", "v0", "v1", "v2", "v3", "v4", "v5", "v6", "v7", "v8", "v9", "v10", "v11", "v12", "v13",
        "v14", "v15", "v16", "v17", "v18", "v19", "v20", "v21", "v22", "v23", "v24", "v25", "v26", "v27", "v28", "v29", "v30");
}

extern "C" void kernel_launch(void* const* d_in, const int* in_sizes, int n_in, void* d_out, int out_size, void* d_ws,
                              size_t ws_size, hipStream_t stream) {
  attn_decode<<<H * NSPLIT, W * 64, 0, stream>>>(d_in[0], d_in[1], d_in[2], d_out, d_ws);
}
```

```cpp
#include <hip/hip_runtime.h>
#include <hip/hip_bf16.h>

constexpr int PACE_NS = 270;
constexpr int SWEEP_T = 124;
#define SWEEP_D "6"

constexpr int H = 32, D = 128, NSPLIT = 8, W = 16;

using bf16x2 = __attribute__((ext_vector_type(2))) __bf16;
using bf16x8 = __attribute__((ext_vector_type(8))) __bf16;
using u32x4 = __attribute__((ext_vector_type(4))) unsigned;
using f32x4 = __attribute__((ext_vector_type(4))) float;
using f32x2 = __attribute__((ext_vector_type(2))) float;
using u64 = unsigned long long;
using u64x4 = __attribute__((ext_vector_type(4))) u64;
typedef __attribute__((address_space(1))) unsigned char gu8;
typedef __attribute__((address_space(1))) unsigned gu32;
typedef __attribute__((address_space(1))) u64 gu64;
typedef __attribute__((address_space(1))) u32x4 gu32x4;
#define RLX_AGENT __ATOMIC_RELAXED, __HIP_MEMORY_SCOPE_AGENT
#define SB() __builtin_amdgcn_sched_barrier(0)

__device__ __forceinline__ float dot2(unsigned k, unsigned q, float acc) {
  return __builtin_amdgcn_fdot2_f32_bf16(__builtin_bit_cast(bf16x2, k), __builtin_bit_cast(bf16x2, q), acc, false);
}
__device__ __forceinline__ unsigned cvtpk(float lo, float hi) {
  bf16x2 r = {(__bf16)lo, (__bf16)hi};
  return __builtin_bit_cast(unsigned, r);
}
template <int CTRL> __device__ __forceinline__ float dpp(float x) {
  return __builtin_bit_cast(float, __builtin_amdgcn_mov_dpp(__builtin_bit_cast(int, x), CTRL, 0xf, 0xf, true));
}
__device__ __forceinline__ float row_sum(float x) {
  x += dpp<0xB1>(x); x += dpp<0x4E>(x); x += dpp<0x141>(x); x += dpp<0x140>(x); return x;
}
__device__ __forceinline__ float xrow_max(float x) {
  auto s = __builtin_amdgcn_permlane16_swap(__float_as_uint(x), __float_as_uint(x), false, false);
  x = fmaxf(__uint_as_float(s[0]), __uint_as_float(s[1]));
  auto t = __builtin_amdgcn_permlane32_swap(__float_as_uint(x), __float_as_uint(x), false, false);
  return fmaxf(__uint_as_float(t[0]), __uint_as_float(t[1]));
}
__device__ __forceinline__ float xrow_sum(float x) {
  auto s = __builtin_amdgcn_permlane16_swap(__float_as_uint(x), __float_as_uint(x), false, false);
  x = __uint_as_float(s[0]) + __uint_as_float(s[1]);
  auto t = __builtin_amdgcn_permlane32_swap(__float_as_uint(x), __float_as_uint(x), false, false);
  return __uint_as_float(t[0]) + __uint_as_float(t[1]);
}
__device__ __forceinline__ unsigned now() { return (unsigned)__builtin_amdgcn_s_memrealtime(); }

constexpr unsigned HEAD_B = 16384, SLOTS_B = 1024, SLOT_B = 640;

__global__ void __launch_bounds__(W * 64, W / 4)
attn_decode(const void* q_, const void* k_, const void* v_, void* out_, void* ws_) {
  __shared__ __attribute__((aligned(16))) float lds[W * D + 2 * W];
  u64x4 a03;
  u64 a4;
  asm volatile("s_load_dwordx8 %0, %2, 0x0\n\ts_load_dwordx2 %1, %2, 0x20"
               : "=&s"(a03), "=&s"(a4) : "s"(__builtin_amdgcn_kernarg_segment_ptr()));
  SB();
  const int b = blockIdx.x;
  const int h = (b & 7) * 4 + ((b >> 3) & 3), s = b >> 5;
  const int tid = threadIdx.x, lane = tid & 63, wave = __builtin_amdgcn_readfirstlane(tid >> 6);
  const int c = lane & 15;
  constexpr float C2 = 0.088388347648318440f * 1.4426950408889634f;

  unsigned so = ((unsigned)h << 20) + (((wave + h) & 15) << 16) + (((s + wave) & 7) << 13) + 4096 + lane * 16;
  unsigned vq = h * 256 + c * 16, vep = h * HEAD_B;
  asm volatile("" : "+v"(so), "+v"(vq), "+v"(vep));
  SB();
  u64 t64;
  asm volatile("s_waitcnt lgkmcnt(0)\n\ts_memrealtime s[40:41]" : "+s"(a03), "+s"(a4), "=&{s[40:41]}"(t64));
  gu8 *const q = (gu8*)a03[0], *const k = (gu8*)a03[1], *const v = (gu8*)a03[2], *const out = (gu8*)a03[3], *const ws = (gu8*)a4;
  const u32x4 qf = *(const gu32x4*)(q + vq);
  const unsigned e0 = __hip_atomic_load((gu32*)(ws + vep), RLX_AGENT);
  SB();
  asm volatile("s_cmp_eq_u32 %[g], 0\n\t"
               "s_cbranch_scc1 2f\n\t"
               "1:\n\t"
               "s_sleep 1\n\t"
               "s_memrealtime s[42:43]\n\t"
               "s_waitcnt lgkmcnt(0)\n\t"
               "s_sub_u32 s42, s42, s40\n\t"
               "s_cmp_lt_u32 s42, %[g]\n\t"
               "s_cbranch_scc1 1b\n\t"
               "2:"
               : "+{s[40:41]}"(t64) : [g] "s"((wave * (PACE_NS * 1024 / 10)) >> 10) : "scc", "s42", "s43");
#define LD(dst, p) _Pragma("unroll") for (int i = 0; i < 8; ++i) { \
    dst[i] = __builtin_nontemporal_load((const gu32x4*)(p + (size_t)so + (i * 1024 - 4096))); SB(); }
  u32x4 kr[8], vr[8], pf[8];
  SB();
  LD(kr, k);
  asm volatile("s_waitcnt lgkmcnt(0)" : "+{s[40:41]}"(t64));
  const unsigned t0 = (unsigned)t64;
  gu8* hw = ws + h * HEAD_B;
  unsigned tag = e0 + 0x9E3779B9u;
  gu64* mine = (gu64*)(hw + SLOTS_B + (((tid >> 7) & 1) * NSPLIT + s) * SLOT_B) + (tid & 127);
  const bool ko = tid < 256 && (tid & 127) < 66;
  if (ko) __hip_atomic_store(mine, (u64)e0 << 32, RLX_AGENT);
  SB();
  if (wave >= W / 2) __builtin_amdgcn_s_barrier();
  SB();

  float x[8];
#pragma unroll
  for (int i = 0; i < 8; ++i) {
    float d = dot2(kr[i][0], qf[0], 0.f);
    d = dot2(kr[i][1], qf[1], d); d = dot2(kr[i][2], qf[2], d); d = dot2(kr[i][3], qf[3], d);
    x[i] = row_sum(d) * C2;
  }
  float m = x[0], l = 0.f;
#pragma unroll
  for (int i = 1; i < 8; ++i) m = fmaxf(m, x[i]);
  m = xrow_max(m);
  const unsigned mlo = c < 8 ? 0u : 0xffff0000u;
  const unsigned half = (c & 1) ? 0xffff0000u : 0x0000ffffu;
  const int dsel = (c & 7) >> 1;
  const unsigned mk0 = dsel == 0 ? half : 0u, mk1 = dsel == 1 ? half : 0u, mk2 = dsel == 2 ? half : 0u, mk3 = dsel == 3 ? half : 0u;
#pragma unroll
  for (int i = 0; i < 8; ++i) {
    const float p = __builtin_amdgcn_exp2f(x[i] - m);
    l += p;
    const unsigned hi = cvtpk(p, p);
    const float r = p - __uint_as_float(hi & mlo);
    const unsigned pk = cvtpk(r, r);
    pf[i] = u32x4{pk & mk0, pk & mk1, pk & mk2, pk & mk3};
    asm volatile("" : "+v"(pf[i]));
  }
  u64 pre = 0;
  if (ko) pre = __hip_atomic_load(mine, RLX_AGENT);
  if (wave < W / 2) __builtin_amdgcn_s_barrier();
  SB();
  {
    const unsigned g = (W + wave) * PACE_NS / 10;
    while (now() - t0 < g - 36) __builtin_amdgcn_s_sleep(8);
    while (now() - t0 < g) __builtin_amdgcn_s_sleep(1);
    SB();
  }
  LD(vr, v);
  l = xrow_sum(l);
  *reinterpret_cast<f32x2*>(lds + W * D + 2 * wave) = f32x2{m, l};
  const gu8* sb = hw + SLOTS_B + (wave & 1) * (NSPLIT * SLOT_B) + 4 * SLOT_B;
  unsigned vo = lane * 8, vme = s * SLOT_B + lane * 8;
  unsigned vml = ((c & 7) == 0 ? 1 : (c & 7)) * SLOT_B + 512 + (c >> 3) * 8;
  unsigned vmy = s * SLOT_B + 512 + (c >> 3) * 8;
  const unsigned lbase = (unsigned)(size_t)(__attribute__((address_space(3))) float*)lds;
  unsigned vrec = lbase + ((wave & 1) * 64 + lane) * 4, vl = lbase + (W * D + 2 * c) * 4, vout = h * 256 + (tid & 127) * 2;
  const u64 own = __ballot((c & 7) == 0), hi8 = __ballot(c >= 8);
  unsigned vw = lbase + (wave * D + 8 * c + 4 * ((lane >> 4) & 1) + 2 * (lane >> 5)) * 4;
  asm volatile("" : "+v"(vo), "+v"(vme), "+v"(vml), "+v"(vmy), "+v"(vrec), "+v"(vl), "+v"(vout), "+v"(vw), "+v"(tag)
               : "s"(out), "s"(sb), "s"(own), "s"(hi8));
  SB();
  f32x4 acc = {0.f, 0.f, 0.f, 0.f};
#pragma unroll
  for (int i = 0; i < 8; ++i)
    acc = __builtin_amdgcn_mfma_f32_16x16x32_bf16(__builtin_bit_cast(bf16x8, pf[i]), __builtin_bit_cast(bf16x8, vr[i]), acc, 0, 0, 0);
  auto s02 = __builtin_amdgcn_permlane32_swap(__float_as_uint(acc[0]), __float_as_uint(acc[2]), false, false);
  auto s13 = __builtin_amdgcn_permlane32_swap(__float_as_uint(acc[1]), __float_as_uint(acc[3]), false, false);
  const f32x2 o2 = {__uint_as_float(s02[0]) + __uint_as_float(s02[1]), __uint_as_float(s13[0]) + __uint_as_float(s13[1])};
  *(__attribute__((address_space(3))) f32x2*)(size_t)vw = o2;
  asm volatile("" :: "v"(pre));
  asm volatile("s_waitcnt vmcnt(0) lgkmcnt(0)\n\ts_barrier" ::: "memory");
  if (wave >= 2) return;

#define DPPM " row_mask:0xf bank_mask:0xf\n\t"
  static_assert(SLOT_B == 640 && W == 16 && NSPLIT == 8, "offsets in the asm");
  asm volatile(
      "ds_read_b64 v[26:27], %[vl]\n\t"
      "ds_read2st64_b32 v[0:1], %[vrec] offset1:2\n\t"
      "ds_read2st64_b32 v[2:3], %[vrec] offset0:4 offset1:6\n\t"
      "ds_read2st64_b32 v[4:5], %[vrec] offset0:8 offset1:10\n\t"
      "ds_read2st64_b32 v[6:7], %[vrec] offset0:12 offset1:14\n\t"
      "ds_read2st64_b32 v[8:9], %[vrec] offset0:16 offset1:18\n\t"
      "ds_read2st64_b32 v[10:11], %[vrec] offset0:20 offset1:22\n\t"
      "ds_read2st64_b32 v[12:13], %[vrec] offset0:24 offset1:26\n\t"
      "ds_read2st64_b32 v[14:15], %[vrec] offset0:28 offset1:30\n\t"
      "s_cmp_eq_u32 %[s], 0\n\t"
      "s_waitcnt lgkmcnt(8)\n\t"
      "v_max_f32_dpp v28, v26, v26 quad_perm:[1,0,3,2]" DPPM
      "v_mov_b32 v25, %[tag]\n\t"
      "v_mov_b32 v29, %[tag]\n\t"
      "v_max_f32_dpp v28, v28, v28 quad_perm:[2,3,0,1]" DPPM
      "v_mov_b32 v30, 0\n\t"
      "s_nop 0\n\t"
      "v_max_f32_dpp v28, v28, v28 row_half_mirror" DPPM
      "s_nop 1\n\t"
      "v_max_f32_dpp v28, v28, v28 row_mirror" DPPM
      "v_sub_f32 v26, v26, v28\n\t"
      "v_exp_f32 v26, v26\n\t"
      "s_waitcnt lgkmcnt(0)\n\t"
      "v_mul_f32 v27, v26, v27\n\t"
      "v_mul_f32_dpp v16, v26, v0 row_newbcast:0" DPPM
      "v_mul_f32_dpp v17, v26, v1 row_newbcast:1" DPPM
      "v_add_f32_dpp v27, v27, v27 quad_perm:[1,0,3,2]" DPPM
      "v_mul_f32_dpp v18, v26, v2 row_newbcast:2" DPPM
      "v_mul_f32_dpp v19, v26, v3 row_newbcast:3" DPPM
      "v_add_f32_dpp v27, v27, v27 quad_perm:[2,3,0,1]" DPPM
      "v_fmac_f32_dpp v16, v26, v4 row_newbcast:4" DPPM
      "v_fmac_f32_dpp v17, v26, v5 row_newbcast:5" DPPM
      "v_fmac_f32_dpp v18, v26, v6 row_newbcast:6" DPPM
      "v_fmac_f32_dpp v19, v26, v7 row_newbcast:7" DPPM
      "v_add_f32_dpp v27, v27, v27 row_half_mirror" DPPM
      "v_fmac_f32_dpp v16, v26, v8 row_newbcast:8" DPPM
      "v_fmac_f32_dpp v17, v26, v9 row_newbcast:9" DPPM
      "v_fmac_f32_dpp v18, v26, v10 row_newbcast:10" DPPM
      "v_fmac_f32_dpp v19, v26, v11 row_newbcast:11" DPPM
      "v_add_f32_dpp v27, v27, v27 row_mirror" DPPM
      "v_fmac_f32_dpp v16, v26, v12 row_newbcast:12" DPPM
      "v_fmac_f32_dpp v17, v26, v13 row_newbcast:13" DPPM
      "v_fmac_f32_dpp v18, v26, v14 row_newbcast:14" DPPM
      "v_fmac_f32_dpp v19, v26, v15 row_newbcast:15" DPPM
      "v_pk_add_f32 v[16:17], v[16:17], v[18:19]\n\t"
      "v_cndmask_b32 v28, v28, v27, %[hi8]\n\t"
      "v_add_f32 v24, v16, v17\n\t"
      "s_cbranch_scc1 2f\n\t"
      "global_store_dwordx2 %[vme], v[24:25], %[sb] offset:-2560 sc1\n\t"
      "global_store_dwordx2 %[vmy], v[28:29], %[sb] offset:-2560 sc1\n\t"
      "s_endpgm\n\t"
      "2:\n\t"
      "s_mov_b32 s44, 0x200000\n\t"
      "s_sleep " SWEEP_D "\n\t"
      "4:\n\t"
      "s_memrealtime s[42:43]\n\t"
      "s_waitcnt lgkmcnt(0)\n\t"
      "s_sub_u32 s42, s42, %[t0]\n\t"
      "s_cmpk_lt_u32 s42, %c[tx]\n\t"
      "s_cbranch_scc1 4b\n\t"
      "1:\n\t"
      "global_load_dwordx2 v[2:3], %[vo], %[sb] offset:-1920 sc1\n\t"
      "global_load_dwordx2 v[4:5], %[vo], %[sb] offset:-1280 sc1\n\t"
      "global_load_dwordx2 v[6:7], %[vo], %[sb] offset:-640 sc1\n\t"
      "global_load_dwordx2 v[8:9], %[vo], %[sb] sc1\n\t"
      "global_load_dwordx2 v[10:11], %[vo], %[sb] offset:640 sc1\n\t"
      "global_load_dwordx2 v[12:13], %[vo], %[sb] offset:1280 sc1\n\t"
      "global_load_dwordx2 v[14:15], %[vo], %[sb] offset:1920 sc1\n\t"
      "global_load_dwordx2 v[0:1], %[vml], %[sb] offset:-2560 sc1\n\t"
      "s_waitcnt vmcnt(0)\n\t"
      "v_cndmask_b32 v20, v0, v28, %[own]\n\t"
      "v_cndmask_b32 v21, v0, v28, %[own]\n\t"
      "v_xor_b32 v22, %[tag], v1\n\t"
      "v_xor_b32 v23, %[tag], v3\n\t"
      "v_mov_b32_dpp v20, v21 row_ror:8 row_mask:0xf bank_mask:0xc\n\t"
      "v_mov_b32_dpp v21, v21 row_ror:8 row_mask:0xf bank_mask:0x3\n\t"
      "v_bitop3_b32 v22, v22, v5, %[tag] bitop3:0xf6\n\t"
      "v_max_f32_dpp v18, v20, v20 quad_perm:[1,0,3,2]" DPPM
      "v_bitop3_b32 v23, v23, v7, %[tag] bitop3:0xf6\n\t"
      "v_bitop3_b32 v22, v22, v9, %[tag] bitop3:0xf6\n\t"
      "v_max_f32_dpp v18, v18, v18 quad_perm:[2,3,0,1]" DPPM
      "v_bitop3_b32 v23, v23, v11, %[tag] bitop3:0xf6\n\t"
      "v_bitop3_b32 v22, v22, v13, %[tag] bitop3:0xf6\n\t"
      "v_max_f32_dpp v18, v18, v18 row_half_mirror" DPPM
      "v_bitop3_b32 v23, v23, v15, %[tag] bitop3:0xf6\n\t"
      "v_sub_f32 v20, v20, v18\n\t"
      "v_exp_f32 v20, v20\n\t"
      "v_or_b32 v22, v22, v23\n\t"
      "v_cmp_ne_u32 vcc, 0, v22\n\t"
      "v_mul_f32 v21, v20, v21\n\t"
      "s_cbranch_vccz 3f\n\t"
      "s_sub_u32 s44, s44, 1\n\t"
      "s_cbranch_scc0 1b\n\t"
      "s_endpgm\n\t"
      "3:\n\t"
      "v_mul_f32_dpp v16, v20, v24 row_newbcast:0" DPPM
      "v_mul_f32_dpp v17, v20, v2 row_newbcast:1" DPPM
      "v_add_f32_dpp v21, v21, v21 quad_perm:[1,0,3,2]" DPPM
      "v_fmac_f32_dpp v16, v20, v4 row_newbcast:2" DPPM
      "v_fmac_f32_dpp v17, v20, v6 row_newbcast:3" DPPM
      "v_add_f32_dpp v21, v21, v21 quad_perm:[2,3,0,1]" DPPM
      "v_fmac_f32_dpp v16, v20, v8 row_newbcast:4" DPPM
      "v_fmac_f32_dpp v17, v20, v10 row_newbcast:5" DPPM
      "v_add_f32_dpp v21, v21, v21 row_half_mirror" DPPM
      "v_fmac_f32_dpp v16, v20, v12 row_newbcast:6" DPPM
      "v_fmac_f32_dpp v17, v20, v14 row_newbcast:7" DPPM
      "v_rcp_f32 v21, v21\n\t"
      "v_add_f32 v16, v16, v17\n\t"
      "v_mul_f32 v16, v16, v21\n\t"
      "v_cvt_pk_bf16_f32 v16, v16, v16\n\t"
      "global_store_short %[vout], v16, %[sout] sc1\n\t"
      "global_store_dword v30, %[tag], %[sep] sc1"
      :
      : [vo] "v"(vo), [vme] "v"(vme), [vml] "v"(vml), [vmy] "v"(vmy), [vrec] "v"(vrec), [vl] "v"(vl), [vout] "v"(vout), [tag] "v"(tag),
        [sb] "s"(sb), [own] "s"(own), [hi8] "s"(hi8), [sout] "s"(out), [sep] "s"(hw), [s] "s"(s),
        [t0] "s"(t0), [tx] "i"((2 * W - 1) * PACE_NS / 10 + SWEEP_T)
      : "memory", "vcc", "scc", "s42", "s43", "s44", "v0", "v1", "v2", "v3", "v4", "v5", "v6", "v7", "v8", "v9", "v10", "v11", "v12", "v13",
        "v14", "v15", "v16", "v17", "v18", "v19", "v20", "v21", "v22", "v23", "v24", "v25", "v26", "v27", "v28", "v29", "v30");
  { const unsigned n0 = now(); int sk = ((int)(n0 - t0) - 960) * 10; if (sk > 5000) sk = 5000; while ((int)(now() - n0) < sk) __builtin_amdgcn_s_sleep(2); }
}

extern "C" void kernel_launch(void* const* d_in, const int* in_sizes, int n_in, void* d_out, int out_size, void* d_ws,
                              size_t ws_size, hipStream_t stream) {
  attn_decode<<<H * NSPLIT, W * 64, 0, stream>>>(d_in[0], d_in[1], d_in[2], d_out, d_ws);
}
```

```cpp
#define PK 272
#define V0 4352
#define PV 272
#define ST 118
#include <hip/hip_runtime.h>
#include <hip/hip_bf16.h>

constexpr int PACEK_NS = PK, V0_NS = V0, PACE_NS = PV;
constexpr int SWEEP_T = ST;
#define SWEEP_D "3"

constexpr int H = 32, D = 128, NSPLIT = 8, W = 16;

using bf16x2 = __attribute__((ext_vector_type(2))) __bf16;
using bf16x8 = __attribute__((ext_vector_type(8))) __bf16;
using u32x4 = __attribute__((ext_vector_type(4))) unsigned;
using f32x4 = __attribute__((ext_vector_type(4))) float;
using f32x2 = __attribute__((ext_vector_type(2))) float;
using u64 = unsigned long long;
using u64x4 = __attribute__((ext_vector_type(4))) u64;
typedef __attribute__((address_space(1))) unsigned char gu8;
typedef __attribute__((address_space(1))) unsigned gu32;
typedef __attribute__((address_space(1))) u64 gu64;
typedef __attribute__((address_space(1))) u32x4 gu32x4;
#define RLX_AGENT __ATOMIC_RELAXED, __HIP_MEMORY_SCOPE_AGENT
#define SB() __builtin_amdgcn_sched_barrier(0)

__device__ __forceinline__ float dot2(unsigned k, unsigned q, float acc) {
  return __builtin_amdgcn_fdot2_f32_bf16(__builtin_bit_cast(bf16x2, k), __builtin_bit_cast(bf16x2, q), acc, false);
}
__device__ __forceinline__ unsigned cvtpk(float lo, float hi) {
  bf16x2 r = {(__bf16)lo, (__bf16)hi};
  return __builtin_bit_cast(unsigned, r);
}
template <int CTRL> __device__ __forceinline__ float dpp(float x) {
  return __builtin_bit_cast(float, __builtin_amdgcn_mov_dpp(__builtin_bit_cast(int, x), CTRL, 0xf, 0xf, true));
}
__device__ __forceinline__ float row_sum(float x) {
  x += dpp<0xB1>(x); x += dpp<0x4E>(x); x += dpp<0x141>(x); x += dpp<0x140>(x); return x;
}
__device__ __forceinline__ float xrow_max(float x) {
  auto s = __builtin_amdgcn_permlane16_swap(__float_as_uint(x), __float_as_uint(x), false, false);
  x = fmaxf(__uint_as_float(s[0]), __uint_as_float(s[1]));
  auto t = __builtin_amdgcn_permlane32_swap(__float_as_uint(x), __float_as_uint(x), false, false);
  return fmaxf(__uint_as_float(t[0]), __uint_as_float(t[1]));
}
__device__ __forceinline__ float xrow_sum(float x) {
  auto s = __builtin_amdgcn_permlane16_swap(__float_as_uint(x), __float_as_uint(x), false, false);
  x = __uint_as_float(s[0]) + __uint_as_float(s[1]);
  auto t = __builtin_amdgcn_permlane32_swap(__float_as_uint(x), __float_as_uint(x), false, false);
  return __uint_as_float(t[0]) + __uint_as_float(t[1]);
}
__device__ __forceinline__ unsigned now() { return (unsigned)__builtin_amdgcn_s_memrealtime(); }

constexpr unsigned HEAD_B = 16384, SLOTS_B = 1024, SLOT_B = 640;

__global__ void __launch_bounds__(W * 64, W / 4)
attn_decode(const void* q_, const void* k_, const void* v_, void* out_, void* ws_) {
  __shared__ __attribute__((aligned(16))) float lds[W * D + 2 * W + 1];
  u64x4 a03;
  u64 a4;
  asm volatile("s_load_dwordx8 %0, %2, 0x0\n\ts_load_dwordx2 %1, %2, 0x20"
               : "=&s"(a03), "=&s"(a4) : "s"(__builtin_amdgcn_kernarg_segment_ptr()));
  SB();
  const int b = blockIdx.x;
  const int h = (b & 7) * 4 + ((b >> 3) & 3), s = b >> 5;
  const int tid = threadIdx.x, lane = tid & 63, wave = __builtin_amdgcn_readfirstlane(tid >> 6);
  const int c = lane & 15;
  constexpr float C2 = 0.088388347648318440f * 1.4426950408889634f;

  unsigned so = ((unsigned)h << 20) + (((wave + h) & 15) << 16) + (((s + wave) & 7) << 13) + 4096 + lane * 16;
  unsigned vq = h * 256 + c * 16, vep = h * HEAD_B;
  asm volatile("" : "+v"(so), "+v"(vq), "+v"(vep));
  SB();
  u64 t64;
  asm volatile("s_waitcnt lgkmcnt(0)\n\ts_memrealtime s[40:41]" : "+s"(a03), "+s"(a4), "=&{s[40:41]}"(t64));
  gu8 *const q = (gu8*)a03[0], *const k = (gu8*)a03[1], *const v = (gu8*)a03[2], *const out = (gu8*)a03[3], *const ws = (gu8*)a4;
  const u32x4 qf = *(const gu32x4*)(q + vq);
  const unsigned e0 = __hip_atomic_load((gu32*)(ws + vep), RLX_AGENT);
  SB();
  asm volatile("s_cmp_eq_u32 %[g], 0\n\t"
               "s_cbranch_scc1 2f\n\t"
               "1:\n\t"
               "s_sleep 1\n\t"
               "s_memrealtime s[42:43]\n\t"
               "s_waitcnt lgkmcnt(0)\n\t"
               "s_sub_u32 s42, s42, s40\n\t"
               "s_cmp_lt_u32 s42, %[g]\n\t"
               "s_cbranch_scc1 1b\n\t"
               "2:"
               : "+{s[40:41]}"(t64) : [g] "s"((wave * (PACEK_NS * 1024 / 10)) >> 10) : "scc", "s42", "s43");
#define LD(dst, p) _Pragma("unroll") for (int i = 0; i < 8; ++i) { \
    dst[i] = __builtin_nontemporal_load((const gu32x4*)(p + (size_t)so + (i * 1024 - 4096))); SB(); }
  u32x4 kr[8], vr[8], pf[8];
  SB();
  LD(kr, k);
  asm volatile("s_waitcnt lgkmcnt(0)" : "+{s[40:41]}"(t64));
  if (wave == 0) lds[W * D + 2 * W] = __uint_as_float((unsigned)t64);
  gu8* hw = ws + h * HEAD_B;
  unsigned tag = e0 + 0x9E3779B9u;
  gu64* mine = (gu64*)(hw + SLOTS_B + (((tid >> 7) & 1) * NSPLIT + s) * SLOT_B) + (tid & 127);
  const bool ko = tid < 256 && (tid & 127) < 66;
  if (ko) __hip_atomic_store(mine, (u64)e0 << 32, RLX_AGENT);
  SB();
  if (wave >= W / 2) asm volatile("s_barrier" ::: "memory");
  SB();

  float x[8];
#pragma unroll
  for (int i = 0; i < 8; ++i) {
    float d = dot2(kr[i][0], qf[0], 0.f);
    d = dot2(kr[i][1], qf[1], d); d = dot2(kr[i][2], qf[2], d); d = dot2(kr[i][3], qf[3], d);
    x[i] = row_sum(d) * C2;
  }
  float m = x[0], l = 0.f;
#pragma unroll
  for (int i = 1; i < 8; ++i) m = fmaxf(m, x[i]);
  m = xrow_max(m);
  const unsigned mlo = c < 8 ? 0u : 0xffff0000u;
  const unsigned half = (c & 1) ? 0xffff0000u : 0x0000ffffu;
  const int dsel = (c & 7) >> 1;
  const unsigned mk0 = dsel == 0 ? half : 0u, mk1 = dsel == 1 ? half : 0u, mk2 = dsel == 2 ? half : 0u, mk3 = dsel == 3 ? half : 0u;
#pragma unroll
  for (int i = 0; i < 8; ++i) {
    const float p = __builtin_amdgcn_exp2f(x[i] - m);
    l += p;
    const unsigned hi = cvtpk(p, p);
    const float r = p - __uint_as_float(hi & mlo);
    const unsigned pk = cvtpk(r, r);
    pf[i] = u32x4{pk & mk0, pk & mk1, pk & mk2, pk & mk3};
    asm volatile("" : "+v"(pf[i]));
  }
  u64 pre = 0;
  if (ko) pre = __hip_atomic_load(mine, RLX_AGENT);
  if (wave < W / 2) asm volatile("s_waitcnt lgkmcnt(0)\n\ts_barrier" ::: "memory");
  const unsigned t0 = __builtin_amdgcn_readfirstlane(__float_as_uint(lds[W * D + 2 * W]));
  SB();
  {
    const unsigned g = (V0_NS + wave * PACE_NS) / 10;
    while (now() - t0 < g - 36) __builtin_amdgcn_s_sleep(8);
    while (now() - t0 < g) __builtin_amdgcn_s_sleep(1);
    SB();
  }
  LD(vr, v);
  l = xrow_sum(l);
  *reinterpret_cast<f32x2*>(lds + W * D + 2 * wave) = f32x2{m, l};
  const gu8* sb = hw + SLOTS_B + (wave & 1) * (NSPLIT * SLOT_B) + 4 * SLOT_B;
  unsigned vo = lane * 8, vme = s * SLOT_B + lane * 8;
  unsigned vml = ((c & 7) == 0 ? 1 : (c & 7)) * SLOT_B + 512 + (c >> 3) * 8;
  unsigned vmy = s * SLOT_B + 512 + (c >> 3) * 8;
  const unsigned lbase = (unsigned)(size_t)(__attribute__((address_space(3))) float*)lds;
  unsigned vrec = lbase + ((wave & 1) * 64 + lane) * 4, vl = lbase + (W * D + 2 * c) * 4, vout = h * 256 + (tid & 127) * 2;
  const u64 own = __ballot((c & 7) == 0), hi8 = __ballot(c >= 8);
  unsigned vw = lbase + (wave * D + 8 * c + 4 * ((lane >> 4) & 1) + 2 * (lane >> 5)) * 4;
  asm volatile("" : "+v"(vo), "+v"(vme), "+v"(vml), "+v"(vmy), "+v"(vrec), "+v"(vl), "+v"(vout), "+v"(vw), "+v"(tag)
               : "s"(out), "s"(sb), "s"(own), "s"(hi8));
  SB();
  f32x4 acc = {0.f, 0.f, 0.f, 0.f};
#pragma unroll
  for (int i = 0; i < 8; ++i)
    acc = __builtin_amdgcn_mfma_f32_16x16x32_bf16(__builtin_bit_cast(bf16x8, pf[i]), __builtin_bit_cast(bf16x8, vr[i]), acc, 0, 0, 0);
  auto s02 = __builtin_amdgcn_permlane32_swap(__float_as_uint(acc[0]), __float_as_uint(acc[2]), false, false);
  auto s13 = __builtin_amdgcn_permlane32_swap(__float_as_uint(acc[1]), __float_as_uint(acc[3]), false, false);
  const f32x2 o2 = {__uint_as_float(s02[0]) + __uint_as_float(s02[1]), __uint_as_float(s13[0]) + __uint_as_float(s13[1])};
  *(__attribute__((address_space(3))) f32x2*)(size_t)vw = o2;
  asm volatile("" :: "v"(pre));
  asm volatile("s_waitcnt vmcnt(0) lgkmcnt(0)\n\ts_barrier" ::: "memory");
  if (wave >= 2) return;

#define DPPM " row_mask:0xf bank_mask:0xf\n\t"
  static_assert(SLOT_B == 640 && W == 16 && NSPLIT == 8, "offsets in the asm");
  asm volatile(
      "ds_read_b64 v[26:27], %[vl]\n\t"
      "ds_read2st64_b32 v[0:1], %[vrec] offset1:2\n\t"
      "ds_read2st64_b32 v[2:3], %[vrec] offset0:4 offset1:6\n\t"
      "ds_read2st64_b32 v[4:5], %[vrec] offset0:8 offset1:10\n\t"
      "ds_read2st64_b32 v[6:7], %[vrec] offset0:12 offset1:14\n\t"
      "ds_read2st64_b32 v[8:9], %[vrec] offset0:16 offset1:18\n\t"
      "ds_read2st64_b32 v[10:11], %[vrec] offset0:20 offset1:22\n\t"
      "ds_read2st64_b32 v[12:13], %[vrec] offset0:24 offset1:26\n\t"
      "ds_read2st64_b32 v[14:15], %[vrec] offset0:28 offset1:30\n\t"
      "s_cmp_eq_u32 %[s], 0\n\t"
      "s_waitcnt lgkmcnt(8)\n\t"
      "v_max_f32_dpp v28, v26, v26 quad_perm:[1,0,3,2]" DPPM
      "v_mov_b32 v25, %[tag]\n\t"
      "v_mov_b32 v29, %[tag]\n\t"
      "v_max_f32_dpp v28, v28, v28 quad_perm:[2,3,0,1]" DPPM
      "v_mov_b32 v30, 0\n\t"
      "s_nop 0\n\t"
      "v_max_f32_dpp v28, v28, v28 row_half_mirror" DPPM
      "s_nop 1\n\t"
      "v_max_f32_dpp v28, v28, v28 row_mirror" DPPM
      "v_sub_f32 v26, v26, v28\n\t"
      "v_exp_f32 v26, v26\n\t"
      "s_waitcnt lgkmcnt(0)\n\t"
      "v_mul_f32 v27, v26, v27\n\t"
      "v_mul_f32_dpp v16, v26, v0 row_newbcast:0" DPPM
      "v_mul_f32_dpp v17, v26, v1 row_newbcast:1" DPPM
      "v_add_f32_dpp v27, v27, v27 quad_perm:[1,0,3,2]" DPPM
      "v_mul_f32_dpp v18, v26, v2 row_newbcast:2" DPPM
      "v_mul_f32_dpp v19, v26, v3 row_newbcast:3" DPPM
      "v_add_f32_dpp v27, v27, v27 quad_perm:[2,3,0,1]" DPPM
      "v_fmac_f32_dpp v16, v26, v4 row_newbcast:4" DPPM
      "v_fmac_f32_dpp v17, v26, v5 row_newbcast:5" DPPM
      "v_fmac_f32_dpp v18, v26, v6 row_newbcast:6" DPPM
      "v_fmac_f32_dpp v19, v26, v7 row_newbcast:7" DPPM
      "v_add_f32_dpp v27, v27, v27 row_half_mirror" DPPM
      "v_fmac_f32_dpp v16, v26, v8 row_newbcast:8" DPPM
      "v_fmac_f32_dpp v17, v26, v9 row_newbcast:9" DPPM
      "v_fmac_f32_dpp v18, v26, v10 row_newbcast:10" DPPM
      "v_fmac_f32_dpp v19, v26, v11 row_newbcast:11" DPPM
      "v_add_f32_dpp v27, v27, v27 row_mirror" DPPM
      "v_fmac_f32_dpp v16, v26, v12 row_newbcast:12" DPPM
      "v_fmac_f32_dpp v17, v26, v13 row_newbcast:13" DPPM
      "v_fmac_f32_dpp v18, v26, v14 row_newbcast:14" DPPM
      "v_fmac_f32_dpp v19, v26, v15 row_newbcast:15" DPPM
      "v_pk_add_f32 v[16:17], v[16:17], v[18:19]\n\t"
      "v_cndmask_b32 v28, v28, v27, %[hi8]\n\t"
      "v_add_f32 v24, v16, v17\n\t"
      "s_cbranch_scc1 2f\n\t"
      "global_store_dwordx2 %[vme], v[24:25], %[sb] offset:-2560 sc1\n\t"
      "global_store_dwordx2 %[vmy], v[28:29], %[sb] offset:-2560 sc1\n\t"
      "s_endpgm\n\t"
      "2:\n\t"
      "s_mov_b32 s44, 0x200000\n\t"
      "s_sleep " SWEEP_D "\n\t"
      "4:\n\t"
      "s_memrealtime s[42:43]\n\t"
      "s_waitcnt lgkmcnt(0)\n\t"
      "s_sub_u32 s42, s42, %[t0]\n\t"
      "s_cmpk_lt_u32 s42, %c[tx]\n\t"
      "s_cbranch_scc1 4b\n\t"
      "1:\n\t"
      "global_load_dwordx2 v[2:3], %[vo], %[sb] offset:-1920 sc1\n\t"
      "global_load_dwordx2 v[4:5], %[vo], %[sb] offset:-1280 sc1\n\t"
      "global_load_dwordx2 v[6:7], %[vo], %[sb] offset:-640 sc1\n\t"
      "global_load_dwordx2 v[8:9], %[vo], %[sb] sc1\n\t"
      "global_load_dwordx2 v[10:11], %[vo], %[sb] offset:640 sc1\n\t"
      "global_load_dwordx2 v[12:13], %[vo], %[sb] offset:1280 sc1\n\t"
      "global_load_dwordx2 v[14:15], %[vo], %[sb] offset:1920 sc1\n\t"
      "global_load_dwordx2 v[0:1], %[vml], %[sb] offset:-2560 sc1\n\t"
      "s_waitcnt vmcnt(0)\n\t"
      "v_cndmask_b32 v20, v0, v28, %[own]\n\t"
      "v_cndmask_b32 v21, v0, v28, %[own]\n\t"
      "v_xor_b32 v22, %[tag], v1\n\t"
      "v_xor_b32 v23, %[tag], v3\n\t"
      "v_mov_b32_dpp v20, v21 row_ror:8 row_mask:0xf bank_mask:0xc\n\t"
      "v_mov_b32_dpp v21, v21 row_ror:8 row_mask:0xf bank_mask:0x3\n\t"
      "v_bitop3_b32 v22, v22, v5, %[tag] bitop3:0xf6\n\t"
      "v_max_f32_dpp v18, v20, v20 quad_perm:[1,0,3,2]" DPPM
      "v_bitop3_b32 v23, v23, v7, %[tag] bitop3:0xf6\n\t"
      "v_bitop3_b32 v22, v22, v9, %[tag] bitop3:0xf6\n\t"
      "v_max_f32_dpp v18, v18, v18 quad_perm:[2,3,0,1]" DPPM
      "v_bitop3_b32 v23, v23, v11, %[tag] bitop3:0xf6\n\t"
      "v_bitop3_b32 v22, v22, v13, %[tag] bitop3:0xf6\n\t"
      "v_max_f32_dpp v18, v18, v18 row_half_mirror" DPPM
      "v_bitop3_b32 v23, v23, v15, %[tag] bitop3:0xf6\n\t"
      "v_sub_f32 v20, v20, v18\n\t"
      "v_exp_f32 v20, v20\n\t"
      "v_or_b32 v22, v22, v23\n\t"
      "v_cmp_ne_u32 vcc, 0, v22\n\t"
      "v_mul_f32 v21, v20, v21\n\t"
      "s_cbranch_vccz 3f\n\t"
      "s_sub_u32 s44, s44, 1\n\t"
      "s_cbranch_scc0 1b\n\t"
      "s_endpgm\n\t"
      "3:\n\t"
      "v_mul_f32_dpp v16, v20, v24 row_newbcast:0" DPPM
      "v_mul_f32_dpp v17, v20, v2 row_newbcast:1" DPPM
      "v_add_f32_dpp v21, v21, v21 quad_perm:[1,0,3,2]" DPPM
      "v_fmac_f32_dpp v16, v20, v4 row_newbcast:2" DPPM
      "v_fmac_f32_dpp v17, v20, v6 row_newbcast:3" DPPM
      "v_add_f32_dpp v21, v21, v21 quad_perm:[2,3,0,1]" DPPM
      "v_fmac_f32_dpp v16, v20, v8 row_newbcast:4" DPPM
      "v_fmac_f32_dpp v17, v20, v10 row_newbcast:5" DPPM
      "v_add_f32_dpp v21, v21, v21 row_half_mirror" DPPM
      "v_fmac_f32_dpp v16, v20, v12 row_newbcast:6" DPPM
      "v_fmac_f32_dpp v17, v20, v14 row_newbcast:7" DPPM
      "v_rcp_f32 v21, v21\n\t"
      "v_add_f32 v16, v16, v17\n\t"
      "v_mul_f32 v16, v16, v21\n\t"
      "v_cvt_pk_bf16_f32 v16, v16, v16\n\t"
      "global_store_short %[vout], v16, %[sout] sc1\n\t"
      "global_store_dword v30, %[tag], %[sep] sc1"
      :
      : [vo] "v"(vo), [vme] "v"(vme), [vml] "v"(vml), [vmy] "v"(vmy), [vrec] "v"(vrec), [vl] "v"(vl), [vout] "v"(vout), [tag] "v"(tag),
        [sb] "s"(sb), [own] "s"(own), [hi8] "s"(hi8), [sout] "s"(out), [sep] "s"(hw), [s] "s"(s),
        [t0] "s"(t0), [tx] "i"((V0_NS + (W - 1) * PACE_NS) / 10 + SWEEP_T)
      : "memory", "vcc", "scc", "s42", "s43", "s44", "v0", "v1", "v2", "v3", "v4", "v5", "v6", "v7", "v8", "v9", "v10", "v11", "v12", "v13",
        "v14", "v15", "v16", "v17", "v18", "v19", "v20", "v21", "v22", "v23", "v24", "v25", "v26", "v27", "v28", "v29", "v30");
  { const unsigned n0 = now(); int sk = ((int)(n0 - t0) - 960) * 10; if (sk > 5000) sk = 5000; while ((int)(now() - n0) < sk) __builtin_amdgcn_s_sleep(2); }
}

extern "C" void kernel_launch(void* const* d_in, const int* in_sizes, int n_in, void* d_out, int out_size, void* d_ws,
                              size_t ws_size, hipStream_t stream) {
  attn_decode<<<H * NSPLIT, W * 64, 0, stream>>>(d_in[0], d_in[1], d_in[2], d_out, d_ws);
}
```

```cpp
#define PK 270
#define V0 4320
#define PV 270
#define ST 116
#include <hip/hip_runtime.h>
#include <hip/hip_bf16.h>

constexpr int PACEK_NS = PK, V0_NS = V0, PACE_NS = PV;
constexpr int SWEEP_T = ST;
#define SWEEP_D "3"

constexpr int H = 32, D = 128, NSPLIT = 8, W = 16;

using bf16x2 = __attribute__((ext_vector_type(2))) __bf16;
using bf16x8 = __attribute__((ext_vector_type(8))) __bf16;
using u32x4 = __attribute__((ext_vector_type(4))) unsigned;
using f32x4 = __attribute__((ext_vector_type(4))) float;
using f32x2 = __attribute__((ext_vector_type(2))) float;
using u64 = unsigned long long;
using u64x4 = __attribute__((ext_vector_type(4))) u64;
typedef __attribute__((address_space(1))) unsigned char gu8;
typedef __attribute__((address_space(1))) unsigned gu32;
typedef __attribute__((address_space(1))) u64 gu64;
typedef __attribute__((address_space(1))) u32x4 gu32x4;
#define RLX_AGENT __ATOMIC_RELAXED, __HIP_MEMORY_SCOPE_AGENT
#define SB() __builtin_amdgcn_sched_barrier(0)

__device__ __forceinline__ float dot2(unsigned k, unsigned q, float acc) {
  return __builtin_amdgcn_fdot2_f32_bf16(__builtin_bit_cast(bf16x2, k), __builtin_bit_cast(bf16x2, q), acc, false);
}
__device__ __forceinline__ unsigned cvtpk(float lo, float hi) {
  bf16x2 r = {(__bf16)lo, (__bf16)hi};
  return __builtin_bit_cast(unsigned, r);
}
template <int CTRL> __device__ __forceinline__ float dpp(float x) {
  return __builtin_bit_cast(float, __builtin_amdgcn_mov_dpp(__builtin_bit_cast(int, x), CTRL, 0xf, 0xf, true));
}
__device__ __forceinline__ float row_sum(float x) {
  x += dpp<0xB1>(x); x += dpp<0x4E>(x); x += dpp<0x141>(x); x += dpp<0x140>(x); return x;
}
__device__ __forceinline__ float xrow_max(float x) {
  auto s = __builtin_amdgcn_permlane16_swap(__float_as_uint(x), __float_as_uint(x), false, false);
  x = fmaxf(__uint_as_float(s[0]), __uint_as_float(s[1]));
  auto t = __builtin_amdgcn_permlane32_swap(__float_as_uint(x), __float_as_uint(x), false, false);
  return fmaxf(__uint_as_float(t[0]), __uint_as_float(t[1]));
}
__device__ __forceinline__ float xrow_sum(float x) {
  auto s = __builtin_amdgcn_permlane16_swap(__float_as_uint(x), __float_as_uint(x), false, false);
  x = __uint_as_float(s[0]) + __uint_as_float(s[1]);
  auto t = __builtin_amdgcn_permlane32_swap(__float_as_uint(x), __float_as_uint(x), false, false);
  return __uint_as_float(t[0]) + __uint_as_float(t[1]);
}
__device__ __forceinline__ unsigned now() { return (unsigned)__builtin_amdgcn_s_memrealtime(); }

constexpr unsigned HEAD_B = 16384, SLOTS_B = 1024, SLOT_B = 640;

__global__ void __launch_bounds__(W * 64, W / 4)
attn_decode(const void* q_, const void* k_, const void* v_, void* out_, void* ws_) {
  __shared__ __attribute__((aligned(16))) float lds[W * D + 2 * W + 1];
  u64x4 a03;
  u64 a4;
  asm volatile("s_load_dwordx8 %0, %2, 0x0\n\ts_load_dwordx2 %1, %2, 0x20"
               : "=&s"(a03), "=&s"(a4) : "s"(__builtin_amdgcn_kernarg_segment_ptr()));
  SB();
  const int b = blockIdx.x;
  const int h = (b & 7) * 4 + ((b >> 3) & 3), s = b >> 5;
  const int tid = threadIdx.x, lane = tid & 63, wave = __builtin_amdgcn_readfirstlane(tid >> 6);
  const int c = lane & 15;
  constexpr float C2 = 0.088388347648318440f * 1.4426950408889634f;

  unsigned so = ((unsigned)h << 20) + (((wave + h) & 15) << 16) + (((s + wave) & 7) << 13) + 4096 + lane * 16;
  unsigned vq = h * 256 + c * 16, vep = h * HEAD_B;
  asm volatile("" : "+v"(so), "+v"(vq), "+v"(vep));
  SB();
  u64 t64;
  asm volatile("s_waitcnt lgkmcnt(0)\n\ts_memrealtime s[40:41]" : "+s"(a03), "+s"(a4), "=&{s[40:41]}"(t64));
  gu8 *const q = (gu8*)a03[0], *const k = (gu8*)a03[1], *const v = (gu8*)a03[2], *const out = (gu8*)a03[3], *const ws = (gu8*)a4;
  const u32x4 qf = *(const gu32x4*)(q + vq);
  const unsigned e0 = __hip_atomic_load((gu32*)(ws + vep), RLX_AGENT);
  SB();
  asm volatile("s_cmp_eq_u32 %[g], 0\n\t"
               "s_cbranch_scc1 2f\n\t"
               "1:\n\t"
               "s_sleep 1\n\t"
               "s_memrealtime s[42:43]\n\t"
               "s_waitcnt lgkmcnt(0)\n\t"
               "s_sub_u32 s42, s42, s40\n\t"
               "s_cmp_lt_u32 s42, %[g]\n\t"
               "s_cbranch_scc1 1b\n\t"
               "2:"
               : "+{s[40:41]}"(t64) : [g] "s"((wave * (PACEK_NS * 1024 / 10)) >> 10) : "scc", "s42", "s43");
#define LD(dst, p) _Pragma("unroll") for (int i = 0; i < 8; ++i) { \
    dst[i] = __builtin_nontemporal_load((const gu32x4*)(p + (size_t)so + (i * 1024 - 4096))); SB(); }
  u32x4 kr[8], vr[8], pf[8];
  SB();
  LD(kr, k);
  asm volatile("s_waitcnt lgkmcnt(0)" : "+{s[40:41]}"(t64));
  if (wave == 0) lds[W * D + 2 * W] = __uint_as_float((unsigned)t64);
  gu8* hw = ws + h * HEAD_B;
  unsigned tag = e0 + 0x9E3779B9u;
  gu64* mine = (gu64*)(hw + SLOTS_B + (((tid >> 7) & 1) * NSPLIT + s) * SLOT_B) + (tid & 127);
  const bool ko = tid < 256 && (tid & 127) < 66;
  if (ko) __hip_atomic_store(mine, (u64)e0 << 32, RLX_AGENT);
  SB();
  if (wave >= W / 2) asm volatile("s_barrier" ::: "memory");
  SB();

  float x[8];
#pragma unroll
  for (int i = 0; i < 8; ++i) {
    float d = dot2(kr[i][0], qf[0], 0.f);
    d = dot2(kr[i][1], qf[1], d); d = dot2(kr[i][2], qf[2], d); d = dot2(kr[i][3], qf[3], d);
    x[i] = row_sum(d) * C2;
  }
  float m = x[0], l = 0.f;
#pragma unroll
  for (int i = 1; i < 8; ++i) m = fmaxf(m, x[i]);
  m = xrow_max(m);
  const unsigned mlo = c < 8 ? 0u : 0xffff0000u;
  const unsigned half = (c & 1) ? 0xffff0000u : 0x0000ffffu;
  const int dsel = (c & 7) >> 1;
  const unsigned mk0 = dsel == 0 ? half : 0u, mk1 = dsel == 1 ? half : 0u, mk2 = dsel == 2 ? half : 0u, mk3 = dsel == 3 ? half : 0u;
#pragma unroll
  for (int i = 0; i < 8; ++i) {
    const float p = __builtin_amdgcn_exp2f(x[i] - m);
    l += p;
    const unsigned hi = cvtpk(p, p);
    const float r = p - __uint_as_float(hi & mlo);
    const unsigned pk = cvtpk(r, r);
    pf[i] = u32x4{pk & mk0, pk & mk1, pk & mk2, pk & mk3};
    asm volatile("" : "+v"(pf[i]));
  }
  u64 pre = 0;
  if (ko) pre = __hip_atomic_load(mine, RLX_AGENT);
  if (wave < W / 2) asm volatile("s_waitcnt lgkmcnt(0)\n\ts_barrier" ::: "memory");
  const unsigned t0 = __builtin_amdgcn_readfirstlane(__float_as_uint(lds[W * D + 2 * W]));
  SB();
  {
    const unsigned g = (V0_NS + wave * PACE_NS) / 10;
    while (now() - t0 < g - 36) __builtin_amdgcn_s_sleep(8);
    while (now() - t0 < g) __builtin_amdgcn_s_sleep(1);
    SB();
  }
  LD(vr, v);
  l = xrow_sum(l);
  *reinterpret_cast<f32x2*>(lds + W * D + 2 * wave) = f32x2{m, l};
  const gu8* sb = hw + SLOTS_B + (wave & 1) * (NSPLIT * SLOT_B) + 4 * SLOT_B;
  unsigned vo = lane * 8, vme = s * SLOT_B + lane * 8;
  unsigned vml = ((c & 7) == 0 ? 1 : (c & 7)) * SLOT_B + 512 + (c >> 3) * 8;
  unsigned vmy = s * SLOT_B + 512 + (c >> 3) * 8;
  const unsigned lbase = (unsigned)(size_t)(__attribute__((address_space(3))) float*)lds;
  unsigned vrec = lbase + ((wave & 1) * 64 + lane) * 4, vl = lbase + (W * D + 2 * c) * 4, vout = h * 256 + (tid & 127) * 2;
  const u64 own = __ballot((c & 7) == 0), hi8 = __ballot(c >= 8);
  unsigned vw = lbase + (wave * D + 8 * c + 4 * ((lane >> 4) & 1) + 2 * (lane >> 5)) * 4;
  asm volatile("" : "+v"(vo), "+v"(vme), "+v"(vml), "+v"(vmy), "+v"(vrec), "+v"(vl), "+v"(vout), "+v"(vw), "+v"(tag)
               : "s"(out), "s"(sb), "s"(own), "s"(hi8));
  SB();
  f32x4 acc = {0.f, 0.f, 0.f, 0.f};
#pragma unroll
  for (int i = 0; i < 8; ++i)
    acc = __builtin_amdgcn_mfma_f32_16x16x32_bf16(__builtin_bit_cast(bf16x8, pf[i]), __builtin_bit_cast(bf16x8, vr[i]), acc, 0, 0, 0);
  auto s02 = __builtin_amdgcn_permlane32_swap(__float_as_uint(acc[0]), __float_as_uint(acc[2]), false, false);
  auto s13 = __builtin_amdgcn_permlane32_swap(__float_as_uint(acc[1]), __float_as_uint(acc[3]), false, false);
  const f32x2 o2 = {__uint_as_float(s02[0]) + __uint_as_float(s02[1]), __uint_as_float(s13[0]) + __uint_as_float(s13[1])};
  *(__attribute__((address_space(3))) f32x2*)(size_t)vw = o2;
  asm volatile("" :: "v"(pre));
  asm volatile("s_waitcnt vmcnt(0) lgkmcnt(0)\n\ts_barrier" ::: "memory");
  if (wave >= 2) return;

#define DPPM " row_mask:0xf bank_mask:0xf\n\t"
  static_assert(SLOT_B == 640 && W == 16 && NSPLIT == 8, "offsets in the asm");
  asm volatile(
      "ds_read_b64 v[26:27], %[vl]\n\t"
      "ds_read2st64_b32 v[0:1], %[vrec] offset1:2\n\t"
      "ds_read2st64_b32 v[2:3], %[vrec] offset0:4 offset1:6\n\t"
      "ds_read2st64_b32 v[4:5], %[vrec] offset0:8 offset1:10\n\t"
      "ds_read2st64_b32 v[6:7], %[vrec] offset0:12 offset1:14\n\t"
      "ds_read2st64_b32 v[8:9], %[vrec] offset0:16 offset1:18\n\t"
      "ds_read2st64_b32 v[10:11], %[vrec] offset0:20 offset1:22\n\t"
      "ds_read2st64_b32 v[12:13], %[vrec] offset0:24 offset1:26\n\t"
      "ds_read2st64_b32 v[14:15], %[vrec] offset0:28 offset1:30\n\t"
      "s_cmp_eq_u32 %[s], 0\n\t"
      "s_waitcnt lgkmcnt(8)\n\t"
      "v_max_f32_dpp v28, v26, v26 quad_perm:[1,0,3,2]" DPPM
      "v_mov_b32 v25, %[tag]\n\t"
      "v_mov_b32 v29, %[tag]\n\t"
      "v_max_f32_dpp v28, v28, v28 quad_perm:[2,3,0,1]" DPPM
      "v_mov_b32 v30, 0\n\t"
      "s_nop 0\n\t"
      "v_max_f32_dpp v28, v28, v28 row_half_mirror" DPPM
      "s_nop 1\n\t"
      "v_max_f32_dpp v28, v28, v28 row_mirror" DPPM
      "v_sub_f32 v26, v26, v28\n\t"
      "v_exp_f32 v26, v26\n\t"
      "s_waitcnt lgkmcnt(0)\n\t"
      "v_mul_f32 v27, v26, v27\n\t"
      "v_mul_f32_dpp v16, v26, v0 row_newbcast:0" DPPM
      "v_mul_f32_dpp v17, v26, v1 row_newbcast:1" DPPM
      "v_add_f32_dpp v27, v27, v27 quad_perm:[1,0,3,2]" DPPM
      "v_mul_f32_dpp v18, v26, v2 row_newbcast:2" DPPM
      "v_mul_f32_dpp v19, v26, v3 row_newbcast:3" DPPM
      "v_add_f32_dpp v27, v27, v27 quad_perm:[2,3,0,1]" DPPM
      "v_fmac_f32_dpp v16, v26, v4 row_newbcast:4" DPPM
      "v_fmac_f32_dpp v17, v26, v5 row_newbcast:5" DPPM
      "v_fmac_f32_dpp v18, v26, v6 row_newbcast:6" DPPM
      "v_fmac_f32_dpp v19, v26, v7 row_newbcast:7" DPPM
      "v_add_f32_dpp v27, v27, v27 row_half_mirror" DPPM
      "v_fmac_f32_dpp v16, v26, v8 row_newbcast:8" DPPM
      "v_fmac_f32_dpp v17, v26, v9 row_newbcast:9" DPPM
      "v_fmac_f32_dpp v18, v26, v10 row_newbcast:10" DPPM
      "v_fmac_f32_dpp v19, v26, v11 row_newbcast:11" DPPM
      "v_add_f32_dpp v27, v27, v27 row_mirror" DPPM
      "v_fmac_f32_dpp v16, v26, v12 row_newbcast:12" DPPM
      "v_fmac_f32_dpp v17, v26, v13 row_newbcast:13" DPPM
      "v_fmac_f32_dpp v18, v26, v14 row_newbcast:14" DPPM
      "v_fmac_f32_dpp v19, v26, v15 row_newbcast:15" DPPM
      "v_pk_add_f32 v[16:17], v[16:17], v[18:19]\n\t"
      "v_cndmask_b32 v28, v28, v27, %[hi8]\n\t"
      "v_add_f32 v24, v16, v17\n\t"
      "s_cbranch_scc1 2f\n\t"
      "global_store_dwordx2 %[vme], v[24:25], %[sb] offset:-2560 sc1\n\t"
      "global_store_dwordx2 %[vmy], v[28:29], %[sb] offset:-2560 sc1\n\t"
      "s_endpgm\n\t"
      "2:\n\t"
      "s_mov_b32 s44, 0x200000\n\t"
      "s_sleep " SWEEP_D "\n\t"
      "4:\n\t"
      "s_memrealtime s[42:43]\n\t"
      "s_waitcnt lgkmcnt(0)\n\t"
      "s_sub_u32 s42, s42, %[t0]\n\t"
      "s_cmpk_lt_u32 s42, %c[tx]\n\t"
      "s_cbranch_scc1 4b\n\t"
      "1:\n\t"
      "global_load_dwordx2 v[2:3], %[vo], %[sb] offset:-1920 sc1\n\t"
      "global_load_dwordx2 v[4:5], %[vo], %[sb] offset:-1280 sc1\n\t"
      "global_load_dwordx2 v[6:7], %[vo], %[sb] offset:-640 sc1\n\t"
      "global_load_dwordx2 v[8:9], %[vo], %[sb] sc1\n\t"
      "global_load_dwordx2 v[10:11], %[vo], %[sb] offset:640 sc1\n\t"
      "global_load_dwordx2 v[12:13], %[vo], %[sb] offset:1280 sc1\n\t"
      "global_load_dwordx2 v[14:15], %[vo], %[sb] offset:1920 sc1\n\t"
      "global_load_dwordx2 v[0:1], %[vml], %[sb] offset:-2560 sc1\n\t"
      "s_waitcnt vmcnt(0)\n\t"
      "v_cndmask_b32 v20, v0, v28, %[own]\n\t"
      "v_cndmask_b32 v21, v0, v28, %[own]\n\t"
      "v_xor_b32 v22, %[tag], v1\n\t"
      "v_xor_b32 v23, %[tag], v3\n\t"
      "v_mov_b32_dpp v20, v21 row_ror:8 row_mask:0xf bank_mask:0xc\n\t"
      "v_mov_b32_dpp v21, v21 row_ror:8 row_mask:0xf bank_mask:0x3\n\t"
      "v_bitop3_b32 v22, v22, v5, %[tag] bitop3:0xf6\n\t"
      "v_max_f32_dpp v18, v20, v20 quad_perm:[1,0,3,2]" DPPM
      "v_bitop3_b32 v23, v23, v7, %[tag] bitop3:0xf6\n\t"
      "v_bitop3_b32 v22, v22, v9, %[tag] bitop3:0xf6\n\t"
      "v_max_f32_dpp v18, v18, v18 quad_perm:[2,3,0,1]" DPPM
      "v_bitop3_b32 v23, v23, v11, %[tag] bitop3:0xf6\n\t"
      "v_bitop3_b32 v22, v22, v13, %[tag] bitop3:0xf6\n\t"
      "v_max_f32_dpp v18, v18, v18 row_half_mirror" DPPM
      "v_bitop3_b32 v23, v23, v15, %[tag] bitop3:0xf6\n\t"
      "v_sub_f32 v20, v20, v18\n\t"
      "v_exp_f32 v20, v20\n\t"
      "v_or_b32 v22, v22, v23\n\t"
      "v_cmp_ne_u32 vcc, 0, v22\n\t"
      "v_mul_f32 v21, v20, v21\n\t"
      "s_cbranch_vccz 3f\n\t"
      "s_sub_u32 s44, s44, 1\n\t"
      "s_cbranch_scc0 1b\n\t"
      "s_endpgm\n\t"
      "3:\n\t"
      "v_mul_f32_dpp v16, v20, v24 row_newbcast:0" DPPM
      "v_mul_f32_dpp v17, v20, v2 row_newbcast:1" DPPM
      "v_add_f32_dpp v21, v21, v21 quad_perm:[1,0,3,2]" DPPM
      "v_fmac_f32_dpp v16, v20, v4 row_newbcast:2" DPPM
      "v_fmac_f32_dpp v17, v20, v6 row_newbcast:3" DPPM
      "v_add_f32_dpp v21, v21, v21 quad_perm:[2,3,0,1]" DPPM
      "v_fmac_f32_dpp v16, v20, v8 row_newbcast:4" DPPM
      "v_fmac_f32_dpp v17, v20, v10 row_newbcast:5" DPPM
      "v_add_f32_dpp v21, v21, v21 row_half_mirror" DPPM
      "v_fmac_f32_dpp v16, v20, v12 row_newbcast:6" DPPM
      "v_fmac_f32_dpp v17, v20, v14 row_newbcast:7" DPPM
      "v_rcp_f32 v21, v21\n\t"
      "v_add_f32 v16, v16, v17\n\t"
      "v_mul_f32 v16, v16, v21\n\t"
      "v_cvt_pk_bf16_f32 v16, v16, v16\n\t"
      "global_store_short %[vout], v16, %[sout] sc1\n\t"
      "global_store_dword v30, %[tag], %[sep] sc1"
      :
      : [vo] "v"(vo), [vme] "v"(vme), [vml] "v"(vml), [vmy] "v"(vmy), [vrec] "v"(vrec), [vl] "v"(vl), [vout] "v"(vout), [tag] "v"(tag),
        [sb] "s"(sb), [own] "s"(own), [hi8] "s"(hi8), [sout] "s"(out), [sep] "s"(hw), [s] "s"(s),
        [t0] "s"(t0), [tx] "i"((V0_NS + (W - 1) * PACE_NS) / 10 + SWEEP_T)
      : "memory", "vcc", "scc", "s42", "s43", "s44", "v0", "v1", "v2", "v3", "v4", "v5", "v6", "v7", "v8", "v9", "v10", "v11", "v12", "v13",
        "v14", "v15", "v16", "v17", "v18", "v19", "v20", "v21", "v22", "v23", "v24", "v25", "v26", "v27", "v28", "v29", "v30");
  { const unsigned n0 = now(); int sk = ((int)(n0 - t0) - 960) * 10; if (sk > 5000) sk = 5000; while ((int)(now() - n0) < sk) __builtin_amdgcn_s_sleep(2); }
}

extern "C" void kernel_launch(void* const* d_in, const int* in_sizes, int n_in, void* d_out, int out_size, void* d_ws,
                              size_t ws_size, hipStream_t stream) {
  attn_decode<<<H * NSPLIT, W * 64, 0, stream>>>(d_in[0], d_in[1], d_in[2], d_out, d_ws);
}
```

```cpp
#define PK 272
#define V0 4352
#define PV 272
#define ST 114
#define SWEEP_P "4"
#define A_END "s_branch 9f\n\t"
#include <hip/hip_runtime.h>
#include <hip/hip_bf16.h>

constexpr int PACEK_NS = PK, V0_NS = V0, PACE_NS = PV;
constexpr int SWEEP_T = ST;
#define SWEEP_D "3"

constexpr int H = 32, D = 128, NSPLIT = 8, W = 16;

using bf16x2 = __attribute__((ext_vector_type(2))) __bf16;
using bf16x8 = __attribute__((ext_vector_type(8))) __bf16;
using u32x4 = __attribute__((ext_vector_type(4))) unsigned;
using f32x4 = __attribute__((ext_vector_type(4))) float;
using f32x2 = __attribute__((ext_vector_type(2))) float;
using u64 = unsigned long long;
using u64x4 = __attribute__((ext_vector_type(4))) u64;
typedef __attribute__((address_space(1))) unsigned char gu8;
typedef __attribute__((address_space(1))) unsigned gu32;
typedef __attribute__((address_space(1))) u64 gu64;
typedef __attribute__((address_space(1))) u32x4 gu32x4;
#define RLX_AGENT __ATOMIC_RELAXED, __HIP_MEMORY_SCOPE_AGENT
#define SB() __builtin_amdgcn_sched_barrier(0)

__device__ __forceinline__ float dot2(unsigned k, unsigned q, float acc) {
  return __builtin_amdgcn_fdot2_f32_bf16(__builtin_bit_cast(bf16x2, k), __builtin_bit_cast(bf16x2, q), acc, false);
}
__device__ __forceinline__ unsigned cvtpk(float lo, float hi) {
  bf16x2 r = {(__bf16)lo, (__bf16)hi};
  return __builtin_bit_cast(unsigned, r);
}
template <int CTRL> __device__ __forceinline__ float dpp(float x) {
  return __builtin_bit_cast(float, __builtin_amdgcn_mov_dpp(__builtin_bit_cast(int, x), CTRL, 0xf, 0xf, true));
}
__device__ __forceinline__ float row_sum(float x) {
  x += dpp<0xB1>(x); x += dpp<0x4E>(x); x += dpp<0x141>(x); x += dpp<0x140>(x); return x;
}
__device__ __forceinline__ float xrow_max(float x) {
  auto s = __builtin_amdgcn_permlane16_swap(__float_as_uint(x), __float_as_uint(x), false, false);
  x = fmaxf(__uint_as_float(s[0]), __uint_as_float(s[1]));
  auto t = __builtin_amdgcn_permlane32_swap(__float_as_uint(x), __float_as_uint(x), false, false);
  return fmaxf(__uint_as_float(t[0]), __uint_as_float(t[1]));
}
__device__ __forceinline__ float xrow_sum(float x) {
  auto s = __builtin_amdgcn_permlane16_swap(__float_as_uint(x), __float_as_uint(x), false, false);
  x = __uint_as_float(s[0]) + __uint_as_float(s[1]);
  auto t = __builtin_amdgcn_permlane32_swap(__float_as_uint(x), __float_as_uint(x), false, false);
  return __uint_as_float(t[0]) + __uint_as_float(t[1]);
}
__device__ __forceinline__ unsigned now() { return (unsigned)__builtin_amdgcn_s_memrealtime(); }

constexpr unsigned HEAD_B = 16384, SLOTS_B = 1024, SLOT_B = 640;

__global__ void __launch_bounds__(W * 64, W / 4)
attn_decode(const void* q_, const void* k_, const void* v_, void* out_, void* ws_) {
  __shared__ __attribute__((aligned(16))) float lds[W * D + 2 * W + 1];
  u64x4 a03;
  u64 a4;
  asm volatile("s_load_dwordx8 %0, %2, 0x0\n\ts_load_dwordx2 %1, %2, 0x20"
               : "=&s"(a03), "=&s"(a4) : "s"(__builtin_amdgcn_kernarg_segment_ptr()));
  SB();
  const int b = blockIdx.x;
  const int h = (b & 7) * 4 + ((b >> 3) & 3), s = b >> 5;
  const int tid = threadIdx.x, lane = tid & 63, wave = __builtin_amdgcn_readfirstlane(tid >> 6);
  const int c = lane & 15;
  constexpr float C2 = 0.088388347648318440f * 1.4426950408889634f;

  unsigned so = ((unsigned)h << 20) + (((wave + h) & 15) << 16) + (((s + wave) & 7) << 13) + 4096 + lane * 16;
  unsigned vq = h * 256 + c * 16, vep = h * HEAD_B;
  asm volatile("" : "+v"(so), "+v"(vq), "+v"(vep));
  SB();
  u64 t64;
  asm volatile("s_waitcnt lgkmcnt(0)\n\ts_memrealtime s[40:41]" : "+s"(a03), "+s"(a4), "=&{s[40:41]}"(t64));
  gu8 *const q = (gu8*)a03[0], *const k = (gu8*)a03[1], *const v = (gu8*)a03[2], *const out = (gu8*)a03[3], *const ws = (gu8*)a4;
  const u32x4 qf = *(const gu32x4*)(q + vq);
  const unsigned e0 = __hip_atomic_load((gu32*)(ws + vep), RLX_AGENT);
  SB();
  asm volatile("s_cmp_eq_u32 %[g], 0\n\t"
               "s_cbranch_scc1 2f\n\t"
               "1:\n\t"
               "s_sleep 1\n\t"
               "s_memrealtime s[42:43]\n\t"
               "s_waitcnt lgkmcnt(0)\n\t"
               "s_sub_u32 s42, s42, s40\n\t"
               "s_cmp_lt_u32 s42, %[g]\n\t"
               "s_cbranch_scc1 1b\n\t"
               "2:"
               : "+{s[40:41]}"(t64) : [g] "s"((wave * (PACEK_NS * 1024 / 10)) >> 10) : "scc", "s42", "s43");
#define LD(dst, p) _Pragma("unroll") for (int i = 0; i < 8; ++i) { \
    dst[i] = __builtin_nontemporal_load((const gu32x4*)(p + (size_t)so + (i * 1024 - 4096))); SB(); }
  u32x4 kr[8], vr[8], pf[8];
  SB();
  LD(kr, k);
  asm volatile("s_waitcnt lgkmcnt(0)" : "+{s[40:41]}"(t64));
  if (wave == 0) lds[W * D + 2 * W] = __uint_as_float((unsigned)t64);
  gu8* hw = ws + h * HEAD_B;
  unsigned tag = e0 + 0x9E3779B9u;
  gu64* mine = (gu64*)(hw + SLOTS_B + (((tid >> 7) & 1) * NSPLIT + s) * SLOT_B) + (tid & 127);
  const bool ko = tid < 256 && (tid & 127) < 66;
  if (ko) __hip_atomic_store(mine, (u64)e0 << 32, RLX_AGENT);
  SB();
  if (wave >= W / 2) asm volatile("s_barrier" ::: "memory");
  SB();

  float x[8];
#pragma unroll
  for (int i = 0; i < 8; ++i) {
    float d = dot2(kr[i][0], qf[0], 0.f);
    d = dot2(kr[i][1], qf[1], d); d = dot2(kr[i][2], qf[2], d); d = dot2(kr[i][3], qf[3], d);
    x[i] = row_sum(d) * C2;
  }
  float m = x[0], l = 0.f;
#pragma unroll
  for (int i = 1; i < 8; ++i) m = fmaxf(m, x[i]);
  m = xrow_max(m);
  const unsigned mlo = c < 8 ? 0u : 0xffff0000u;
  const unsigned half = (c & 1) ? 0xffff0000u : 0x0000ffffu;
  const int dsel = (c & 7) >> 1;
  const unsigned mk0 = dsel == 0 ? half : 0u, mk1 = dsel == 1 ? half : 0u, mk2 = dsel == 2 ? half : 0u, mk3 = dsel == 3 ? half : 0u;
#pragma unroll
  for (int i = 0; i < 8; ++i) {
    const float p = __builtin_amdgcn_exp2f(x[i] - m);
    l += p;
    const unsigned hi = cvtpk(p, p);
    const float r = p - __uint_as_float(hi & mlo);
    const unsigned pk = cvtpk(r, r);
    pf[i] = u32x4{pk & mk0, pk & mk1, pk & mk2, pk & mk3};
    asm volatile("" : "+v"(pf[i]));
  }
  u64 pre = 0;
  if (ko) pre = __hip_atomic_load(mine, RLX_AGENT);
  if (wave < W / 2) asm volatile("s_waitcnt lgkmcnt(0)\n\ts_barrier" ::: "memory");
  const unsigned t0 = __builtin_amdgcn_readfirstlane(__float_as_uint(lds[W * D + 2 * W]));
  SB();
  {
    const unsigned g = (V0_NS + wave * PACE_NS) / 10;
    while (now() - t0 < g - 36) __builtin_amdgcn_s_sleep(8);
    while (now() - t0 < g) __builtin_amdgcn_s_sleep(1);
    SB();
  }
  LD(vr, v);
  l = xrow_sum(l);
  *reinterpret_cast<f32x2*>(lds + W * D + 2 * wave) = f32x2{m, l};
  const gu8* sb = hw + SLOTS_B + (wave & 1) * (NSPLIT * SLOT_B) + 4 * SLOT_B;
  unsigned vo = lane * 8, vme = s * SLOT_B + lane * 8;
  unsigned vml = ((c & 7) == 0 ? 1 : (c & 7)) * SLOT_B + 512 + (c >> 3) * 8;
  unsigned vmy = s * SLOT_B + 512 + (c >> 3) * 8;
  const unsigned lbase = (unsigned)(size_t)(__attribute__((address_space(3))) float*)lds;
  unsigned vrec = lbase + ((wave & 1) * 64 + lane) * 4, vl = lbase + (W * D + 2 * c) * 4, vout = h * 256 + (tid & 127) * 2;
  const u64 own = __ballot((c & 7) == 0), hi8 = __ballot(c >= 8);
  unsigned vw = lbase + (wave * D + 8 * c + 4 * ((lane >> 4) & 1) + 2 * (lane >> 5)) * 4;
  asm volatile("" : "+v"(vo), "+v"(vme), "+v"(vml), "+v"(vmy), "+v"(vrec), "+v"(vl), "+v"(vout), "+v"(vw), "+v"(tag)
               : "s"(out), "s"(sb), "s"(own), "s"(hi8));
  SB();
  f32x4 acc = {0.f, 0.f, 0.f, 0.f};
#pragma unroll
  for (int i = 0; i < 8; ++i)
    acc = __builtin_amdgcn_mfma_f32_16x16x32_bf16(__builtin_bit_cast(bf16x8, pf[i]), __builtin_bit_cast(bf16x8, vr[i]), acc, 0, 0, 0);
  auto s02 = __builtin_amdgcn_permlane32_swap(__float_as_uint(acc[0]), __float_as_uint(acc[2]), false, false);
  auto s13 = __builtin_amdgcn_permlane32_swap(__float_as_uint(acc[1]), __float_as_uint(acc[3]), false, false);
  const f32x2 o2 = {__uint_as_float(s02[0]) + __uint_as_float(s02[1]), __uint_as_float(s13[0]) + __uint_as_float(s13[1])};
  *(__attribute__((address_space(3))) f32x2*)(size_t)vw = o2;
  asm volatile("" :: "v"(pre));
  asm volatile("s_waitcnt vmcnt(0) lgkmcnt(0)\n\ts_barrier" ::: "memory");
  if (wave >= 2) return;

#define DPPM " row_mask:0xf bank_mask:0xf\n\t"
#define A_SWEEP(R) \
      "global_load_dwordx2 v[" #R "+2:" #R "+3], %[vo], %[sb] offset:-1920 sc1\n\t" \
      "global_load_dwordx2 v[" #R "+4:" #R "+5], %[vo], %[sb] offset:-1280 sc1\n\t" \
      "global_load_dwordx2 v[" #R "+6:" #R "+7], %[vo], %[sb] offset:-640 sc1\n\t" \
      "global_load_dwordx2 v[" #R "+8:" #R "+9], %[vo], %[sb] sc1\n\t" \
      "global_load_dwordx2 v[" #R "+10:" #R "+11], %[vo], %[sb] offset:640 sc1\n\t" \
      "global_load_dwordx2 v[" #R "+12:" #R "+13], %[vo], %[sb] offset:1280 sc1\n\t" \
      "global_load_dwordx2 v[" #R "+14:" #R "+15], %[vo], %[sb] offset:1920 sc1\n\t" \
      "global_load_dwordx2 v[" #R ":" #R "+1], %[vml], %[sb] offset:-2560 sc1\n\t"
#define A_TRY(R, LBL) \
      "s_waitcnt vmcnt(8)\n\t" \
      "v_cndmask_b32 v20, v[" #R "], v28, %[own]\n\t" \
      "v_cndmask_b32 v21, v[" #R "], v28, %[own]\n\t" \
      "v_xor_b32 v22, %[tag], v[" #R "+1]\n\t" \
      "v_xor_b32 v23, %[tag], v[" #R "+3]\n\t" \
      "v_mov_b32_dpp v20, v21 row_ror:8 row_mask:0xf bank_mask:0xc\n\t"     \
      "v_mov_b32_dpp v21, v21 row_ror:8 row_mask:0xf bank_mask:0x3\n\t"     \
      "v_bitop3_b32 v22, v22, v[" #R "+5], %[tag] bitop3:0xf6\n\t"          \
      "v_max_f32_dpp v18, v20, v20 quad_perm:[1,0,3,2]" DPPM \
      "v_bitop3_b32 v23, v23, v[" #R "+7], %[tag] bitop3:0xf6\n\t" \
      "v_bitop3_b32 v22, v22, v[" #R "+9], %[tag] bitop3:0xf6\n\t" \
      "v_max_f32_dpp v18, v18, v18 quad_perm:[2,3,0,1]" DPPM \
      "v_bitop3_b32 v23, v23, v[" #R "+11], %[tag] bitop3:0xf6\n\t" \
      "v_bitop3_b32 v22, v22, v[" #R "+13], %[tag] bitop3:0xf6\n\t" \
      "v_max_f32_dpp v18, v18, v18 row_half_mirror" DPPM \
      "v_bitop3_b32 v23, v23, v[" #R "+15], %[tag] bitop3:0xf6\n\t" \
      "v_sub_f32 v20, v20, v18\n\t" \
      "v_exp_f32 v20, v20\n\t"                                              \
      "v_or_b32 v22, v22, v23\n\t" \
      "v_cmp_ne_u32 vcc, 0, v22\n\t" \
      "v_mul_f32 v21, v20, v21\n\t" \
      "s_cbranch_vccnz " LBL "f\n\t" \
      "v_mul_f32_dpp v16, v20, v24 row_newbcast:0" DPPM \
      "v_mul_f32_dpp v17, v20, v[" #R "+2] row_newbcast:1" DPPM \
      "v_add_f32_dpp v21, v21, v21 quad_perm:[1,0,3,2]" DPPM \
      "v_fmac_f32_dpp v16, v20, v[" #R "+4] row_newbcast:2" DPPM \
      "v_fmac_f32_dpp v17, v20, v[" #R "+6] row_newbcast:3" DPPM \
      "v_add_f32_dpp v21, v21, v21 quad_perm:[2,3,0,1]" DPPM \
      "v_fmac_f32_dpp v16, v20, v[" #R "+8] row_newbcast:4" DPPM \
      "v_fmac_f32_dpp v17, v20, v[" #R "+10] row_newbcast:5" DPPM \
      "v_add_f32_dpp v21, v21, v21 row_half_mirror" DPPM \
      "v_fmac_f32_dpp v16, v20, v[" #R "+12] row_newbcast:6" DPPM \
      "v_fmac_f32_dpp v17, v20, v[" #R "+14] row_newbcast:7" DPPM \
      "v_rcp_f32 v21, v21\n\t" \
      "v_add_f32 v16, v16, v17\n\t" \
      "v_mul_f32 v16, v16, v21\n\t" \
      "v_cvt_pk_bf16_f32 v16, v16, v16\n\t" \
      "global_store_short %[vout], v16, %[sout] sc1\n\t" \
      "global_store_dword v30, %[tag], %[sep] sc1\n\t" \
      A_END \
      LBL ":\n\t"
  static_assert(SLOT_B == 640 && W == 16 && NSPLIT == 8, "offsets in the asm");
  asm volatile(
      "ds_read_b64 v[26:27], %[vl]\n\t"
      "ds_read2st64_b32 v[0:1], %[vrec] offset1:2\n\t"
      "ds_read2st64_b32 v[2:3], %[vrec] offset0:4 offset1:6\n\t"
      "ds_read2st64_b32 v[4:5], %[vrec] offset0:8 offset1:10\n\t"
      "ds_read2st64_b32 v[6:7], %[vrec] offset0:12 offset1:14\n\t"
      "ds_read2st64_b32 v[8:9], %[vrec] offset0:16 offset1:18\n\t"
      "ds_read2st64_b32 v[10:11], %[vrec] offset0:20 offset1:22\n\t"
      "ds_read2st64_b32 v[12:13], %[vrec] offset0:24 offset1:26\n\t"
      "ds_read2st64_b32 v[14:15], %[vrec] offset0:28 offset1:30\n\t"
      "s_cmp_eq_u32 %[s], 0\n\t"
      "s_waitcnt lgkmcnt(8)\n\t"
      "v_max_f32_dpp v28, v26, v26 quad_perm:[1,0,3,2]" DPPM
      "v_mov_b32 v25, %[tag]\n\t"
      "v_mov_b32 v29, %[tag]\n\t"
      "v_max_f32_dpp v28, v28, v28 quad_perm:[2,3,0,1]" DPPM
      "v_mov_b32 v30, 0\n\t"
      "s_nop 0\n\t"
      "v_max_f32_dpp v28, v28, v28 row_half_mirror" DPPM
      "s_nop 1\n\t"
      "v_max_f32_dpp v28, v28, v28 row_mirror" DPPM
      "v_sub_f32 v26, v26, v28\n\t"
      "v_exp_f32 v26, v26\n\t"
      "s_waitcnt lgkmcnt(0)\n\t"
      "v_mul_f32 v27, v26, v27\n\t"
      "v_mul_f32_dpp v16, v26, v0 row_newbcast:0" DPPM
      "v_mul_f32_dpp v17, v26, v1 row_newbcast:1" DPPM
      "v_add_f32_dpp v27, v27, v27 quad_perm:[1,0,3,2]" DPPM
      "v_mul_f32_dpp v18, v26, v2 row_newbcast:2" DPPM
      "v_mul_f32_dpp v19, v26, v3 row_newbcast:3" DPPM
      "v_add_f32_dpp v27, v27, v27 quad_perm:[2,3,0,1]" DPPM
      "v_fmac_f32_dpp v16, v26, v4 row_newbcast:4" DPPM
      "v_fmac_f32_dpp v17, v26, v5 row_newbcast:5" DPPM
      "v_fmac_f32_dpp v18, v26, v6 row_newbcast:6" DPPM
      "v_fmac_f32_dpp v19, v26, v7 row_newbcast:7" DPPM
      "v_add_f32_dpp v27, v27, v27 row_half_mirror" DPPM
      "v_fmac_f32_dpp v16, v26, v8 row_newbcast:8" DPPM
      "v_fmac_f32_dpp v17, v26, v9 row_newbcast:9" DPPM
      "v_fmac_f32_dpp v18, v26, v10 row_newbcast:10" DPPM
      "v_fmac_f32_dpp v19, v26, v11 row_newbcast:11" DPPM
      "v_add_f32_dpp v27, v27, v27 row_mirror" DPPM
      "v_fmac_f32_dpp v16, v26, v12 row_newbcast:12" DPPM
      "v_fmac_f32_dpp v17, v26, v13 row_newbcast:13" DPPM
      "v_fmac_f32_dpp v18, v26, v14 row_newbcast:14" DPPM
      "v_fmac_f32_dpp v19, v26, v15 row_newbcast:15" DPPM
      "v_pk_add_f32 v[16:17], v[16:17], v[18:19]\n\t"
      "v_cndmask_b32 v28, v28, v27, %[hi8]\n\t"
      "v_add_f32 v24, v16, v17\n\t"
      "s_cbranch_scc1 2f\n\t"
      "global_store_dwordx2 %[vme], v[24:25], %[sb] offset:-2560 sc1\n\t"
      "global_store_dwordx2 %[vmy], v[28:29], %[sb] offset:-2560 sc1\n\t"
      "s_endpgm\n\t"
      "2:\n\t"
      "s_mov_b32 s44, 0x200000\n\t"
      "s_sleep " SWEEP_D "\n\t"
      "4:\n\t"
      "s_memrealtime s[42:43]\n\t"
      "s_waitcnt lgkmcnt(0)\n\t"
      "s_sub_u32 s42, s42, %[t0]\n\t"
      "s_cmpk_lt_u32 s42, %c[tx]\n\t"
      "s_cbranch_scc1 4b\n\t"
      A_SWEEP(0)
      "s_sleep " SWEEP_P "\n\t"
      A_SWEEP(32)
      "1:\n\t"
      A_TRY(0, "5") A_SWEEP(0)
      A_TRY(32, "6") A_SWEEP(32)
      "s_sub_u32 s44, s44, 1\n\t"
      "s_cbranch_scc0 1b\n\t"
      "9:"
      :
      : [vo] "v"(vo), [vme] "v"(vme), [vml] "v"(vml), [vmy] "v"(vmy), [vrec] "v"(vrec), [vl] "v"(vl), [vout] "v"(vout), [tag] "v"(tag),
        [sb] "s"(sb), [own] "s"(own), [hi8] "s"(hi8), [sout] "s"(out), [sep] "s"(hw), [s] "s"(s),
        [t0] "s"(t0), [tx] "i"((V0_NS + (W - 1) * PACE_NS) / 10 + SWEEP_T)
      : "memory", "vcc", "scc", "s42", "s43", "s44", "v0", "v1", "v2", "v3", "v4", "v5", "v6", "v7", "v8", "v9", "v10", "v11", "v12", "v13",
        "v14", "v15", "v16", "v17", "v18", "v19", "v20", "v21", "v22", "v23", "v24", "v25", "v26", "v27", "v28", "v29", "v30",
        "v32", "v33", "v34", "v35", "v36", "v37", "v38", "v39", "v40", "v41", "v42", "v43", "v44", "v45", "v46", "v47");
  { const unsigned n0 = now(); int sk = ((int)(n0 - t0) - 960) * 10; if (sk > 5000) sk = 5000; while ((int)(now() - n0) < sk) __builtin_amdgcn_s_sleep(2); }
}

extern "C" void kernel_launch(void* const* d_in, const int* in_sizes, int n_in, void* d_out, int out_size, void* d_ws,
                              size_t ws_size, hipStream_t stream) {
  attn_decode<<<H * NSPLIT, W * 64, 0, stream>>>(d_in[0], d_in[1], d_in[2], d_out, d_ws);
}
```

```cpp
#include <hip/hip_runtime.h>
#include <hip/hip_bf16.h>

constexpr int PACE_NS = 270;
constexpr int SWEEP_T = 114;
#define SWEEP_D "3"
#define SWEEP_P "4"

constexpr int H = 32, D = 128, NSPLIT = 8, W = 16;

using bf16x2 = __attribute__((ext_vector_type(2))) __bf16;
using bf16x8 = __attribute__((ext_vector_type(8))) __bf16;
using u32x4 = __attribute__((ext_vector_type(4))) unsigned;
using f32x4 = __attribute__((ext_vector_type(4))) float;
using f32x2 = __attribute__((ext_vector_type(2))) float;
using u64 = unsigned long long;
using u64x4 = __attribute__((ext_vector_type(4))) u64;
typedef __attribute__((address_space(1))) unsigned char gu8;
typedef __attribute__((address_space(1))) unsigned gu32;
typedef __attribute__((address_space(1))) u64 gu64;
typedef __attribute__((address_space(1))) u32x4 gu32x4;
#define RLX_AGENT __ATOMIC_RELAXED, __HIP_MEMORY_SCOPE_AGENT
#define SB() __builtin_amdgcn_sched_barrier(0)

__device__ __forceinline__ float dot2(unsigned k, unsigned q, float acc) {
  return __builtin_amdgcn_fdot2_f32_bf16(__builtin_bit_cast(bf16x2, k), __builtin_bit_cast(bf16x2, q), acc, false);
}
__device__ __forceinline__ unsigned cvtpk(float lo, float hi) {
  bf16x2 r = {(__bf16)lo, (__bf16)hi};
  return __builtin_bit_cast(unsigned, r);
}
template <int CTRL> __device__ __forceinline__ float dpp(float x) {
  return __builtin_bit_cast(float, __builtin_amdgcn_mov_dpp(__builtin_bit_cast(int, x), CTRL, 0xf, 0xf, true));
}
__device__ __forceinline__ float row_sum(float x) {
  x += dpp<0xB1>(x); x += dpp<0x4E>(x); x += dpp<0x141>(x); x += dpp<0x140>(x); return x;
}
__device__ __forceinline__ float xrow_max(float x) {
  auto s = __builtin_amdgcn_permlane16_swap(__float_as_uint(x), __float_as_uint(x), false, false);
  x = fmaxf(__uint_as_float(s[0]), __uint_as_float(s[1]));
  auto t = __builtin_amdgcn_permlane32_swap(__float_as_uint(x), __float_as_uint(x), false, false);
  return fmaxf(__uint_as_float(t[0]), __uint_as_float(t[1]));
}
__device__ __forceinline__ float xrow_sum(float x) {
  auto s = __builtin_amdgcn_permlane16_swap(__float_as_uint(x), __float_as_uint(x), false, false);
  x = __uint_as_float(s[0]) + __uint_as_float(s[1]);
  auto t = __builtin_amdgcn_permlane32_swap(__float_as_uint(x), __float_as_uint(x), false, false);
  return __uint_as_float(t[0]) + __uint_as_float(t[1]);
}
__device__ __forceinline__ unsigned now() { return (unsigned)__builtin_amdgcn_s_memrealtime(); }

constexpr unsigned HEAD_B = 16384, SLOTS_B = 1024, SLOT_B = 640;

__global__ void __launch_bounds__(W * 64, W / 4)
attn_decode(const void* q_, const void* k_, const void* v_, void* out_, void* ws_) {
  __shared__ __attribute__((aligned(16))) float lds[W * D + 2 * W + 1];
  u64x4 a03;
  u64 a4;
  asm volatile("s_load_dwordx8 %0, %2, 0x0\n\ts_load_dwordx2 %1, %2, 0x20"
               : "=&s"(a03), "=&s"(a4) : "s"(__builtin_amdgcn_kernarg_segment_ptr()));
  SB();
  const int b = blockIdx.x;
  const int h = (b & 7) * 4 + ((b >> 3) & 3), s = b >> 5;
  const int tid = threadIdx.x, lane = tid & 63, wave = __builtin_amdgcn_readfirstlane(tid >> 6);
  const int c = lane & 15;
  constexpr float C2 = 0.088388347648318440f * 1.4426950408889634f;

  unsigned so = ((unsigned)h << 20) + (((wave + h) & 15) << 16) + (((s + wave) & 7) << 13) + 4096 + lane * 16;
  unsigned vq = h * 256 + c * 16, vep = h * HEAD_B;
  asm volatile("" : "+v"(so), "+v"(vq), "+v"(vep));
  SB();
  u64 t64;
  asm volatile("s_waitcnt lgkmcnt(0)\n\ts_memrealtime s[40:41]" : "+s"(a03), "+s"(a4), "=&{s[40:41]}"(t64));
  gu8 *const q = (gu8*)a03[0], *const k = (gu8*)a03[1], *const v = (gu8*)a03[2], *const out = (gu8*)a03[3], *const ws = (gu8*)a4;
  const u32x4 qf = *(const gu32x4*)(q + vq);
  const unsigned e0 = __hip_atomic_load((gu32*)(ws + vep), RLX_AGENT);
  SB();
  asm volatile("s_cmp_eq_u32 %[g], 0\n\t"
               "s_cbranch_scc1 2f\n\t"
               "1:\n\t"
               "s_sleep 1\n\t"
               "s_memrealtime s[42:43]\n\t"
               "s_waitcnt lgkmcnt(0)\n\t"
               "s_sub_u32 s42, s42, s40\n\t"
               "s_cmp_lt_u32 s42, %[g]\n\t"
               "s_cbranch_scc1 1b\n\t"
               "2:"
               : "+{s[40:41]}"(t64) : [g] "s"((wave * (PACE_NS * 1024 / 10)) >> 10) : "scc", "s42", "s43");
#define LD(dst, p) _Pragma("unroll") for (int i = 0; i < 8; ++i) { \
    dst[i] = __builtin_nontemporal_load((const gu32x4*)(p + (size_t)so + (i * 1024 - 4096))); SB(); }
  u32x4 kr[8], vr[8], pf[8];
  SB();
  LD(kr, k);
  asm volatile("s_waitcnt lgkmcnt(0)" : "+{s[40:41]}"(t64));
  if (wave == 0) lds[W * D + 2 * W] = __uint_as_float((unsigned)t64);
  gu8* hw = ws + h * HEAD_B;
  unsigned tag = e0 + 0x9E3779B9u;
  gu64* mine = (gu64*)(hw + SLOTS_B + (((tid >> 7) & 1) * NSPLIT + s) * SLOT_B) + (tid & 127);
  const bool ko = tid < 256 && (tid & 127) < 66;
  if (ko) __hip_atomic_store(mine, (u64)e0 << 32, RLX_AGENT);
  SB();
  if (wave >= W / 2) asm volatile("s_barrier" ::: "memory");
  SB();

  float x[8];
#pragma unroll
  for (int i = 0; i < 8; ++i) {
    float d = dot2(kr[i][0], qf[0], 0.f);
    d = dot2(kr[i][1], qf[1], d); d = dot2(kr[i][2], qf[2], d); d = dot2(kr[i][3], qf[3], d);
    x[i] = row_sum(d) * C2;
  }
  float m = x[0], l = 0.f;
#pragma unroll
  for (int i = 1; i < 8; ++i) m = fmaxf(m, x[i]);
  m = xrow_max(m);
  const unsigned mlo = c < 8 ? 0u : 0xffff0000u;
  const unsigned half = (c & 1) ? 0xffff0000u : 0x0000ffffu;
  const int dsel = (c & 7) >> 1;
  const unsigned mk0 = dsel == 0 ? half : 0u, mk1 = dsel == 1 ? half : 0u, mk2 = dsel == 2 ? half : 0u, mk3 = dsel == 3 ? half : 0u;
#pragma unroll
  for (int i = 0; i < 8; ++i) {
    const float p = __builtin_amdgcn_exp2f(x[i] - m);
    l += p;
    const unsigned hi = cvtpk(p, p);
    const float r = p - __uint_as_float(hi & mlo);
    const unsigned pk = cvtpk(r, r);
    pf[i] = u32x4{pk & mk0, pk & mk1, pk & mk2, pk & mk3};
    asm volatile("" : "+v"(pf[i]));
  }
  u64 pre = 0;
  if (ko) pre = __hip_atomic_load(mine, RLX_AGENT);
  if (wave < W / 2) asm volatile("s_waitcnt lgkmcnt(0)\n\ts_barrier" ::: "memory");
  const unsigned t0 = __builtin_amdgcn_readfirstlane(__float_as_uint(lds[W * D + 2 * W]));
  SB();
  {
    const unsigned g = (W + wave) * PACE_NS / 10;
    while (now() - t0 < g - 36) __builtin_amdgcn_s_sleep(8);
    while (now() - t0 < g) __builtin_amdgcn_s_sleep(1);
    SB();
  }
  LD(vr, v);
  l = xrow_sum(l);
  *reinterpret_cast<f32x2*>(lds + W * D + 2 * wave) = f32x2{m, l};
  const gu8* sb = hw + SLOTS_B + (wave & 1) * (NSPLIT * SLOT_B) + 4 * SLOT_B;
  unsigned vo = lane * 8, vme = s * SLOT_B + lane * 8;
  unsigned vml = ((c & 7) == 0 ? 1 : (c & 7)) * SLOT_B + 512 + (c >> 3) * 8;
  unsigned vmy = s * SLOT_B + 512 + (c >> 3) * 8;
  const unsigned lbase = (unsigned)(size_t)(__attribute__((address_space(3))) float*)lds;
  unsigned vrec = lbase + ((wave & 1) * 64 + lane) * 4, vl = lbase + (W * D + 2 * c) * 4, vout = h * 256 + (tid & 127) * 2;
  const u64 own = __ballot((c & 7) == 0), hi8 = __ballot(c >= 8);
  unsigned vw = lbase + (wave * D + 8 * c + 4 * ((lane >> 4) & 1) + 2 * (lane >> 5)) * 4;
  asm volatile("" : "+v"(vo), "+v"(vme), "+v"(vml), "+v"(vmy), "+v"(vrec), "+v"(vl), "+v"(vout), "+v"(vw), "+v"(tag)
               : "s"(out), "s"(sb), "s"(own), "s"(hi8));
  SB();
  f32x4 acc = {0.f, 0.f, 0.f, 0.f};
#pragma unroll
  for (int i = 0; i < 8; ++i)
    acc = __builtin_amdgcn_mfma_f32_16x16x32_bf16(__builtin_bit_cast(bf16x8, pf[i]), __builtin_bit_cast(bf16x8, vr[i]), acc, 0, 0, 0);
  auto s02 = __builtin_amdgcn_permlane32_swap(__float_as_uint(acc[0]), __float_as_uint(acc[2]), false, false);
  auto s13 = __builtin_amdgcn_permlane32_swap(__float_as_uint(acc[1]), __float_as_uint(acc[3]), false, false);
  const f32x2 o2 = {__uint_as_float(s02[0]) + __uint_as_float(s02[1]), __uint_as_float(s13[0]) + __uint_as_float(s13[1])};
  *(__attribute__((address_space(3))) f32x2*)(size_t)vw = o2;
  asm volatile("" :: "v"(pre));
  asm volatile("s_waitcnt vmcnt(0) lgkmcnt(0)\n\ts_barrier" ::: "memory");
  if (wave >= 2) return;

#define DPPM " row_mask:0xf bank_mask:0xf\n\t"
#define A_SWEEP(R) \
      "global_load_dwordx2 v[" #R "+2:" #R "+3], %[vo], %[sb] offset:-1920 sc1\n\t" \
      "global_load_dwordx2 v[" #R "+4:" #R "+5], %[vo], %[sb] offset:-1280 sc1\n\t" \
      "global_load_dwordx2 v[" #R "+6:" #R "+7], %[vo], %[sb] offset:-640 sc1\n\t" \
      "global_load_dwordx2 v[" #R "+8:" #R "+9], %[vo], %[sb] sc1\n\t" \
      "global_load_dwordx2 v[" #R "+10:" #R "+11], %[vo], %[sb] offset:640 sc1\n\t" \
      "global_load_dwordx2 v[" #R "+12:" #R "+13], %[vo], %[sb] offset:1280 sc1\n\t" \
      "global_load_dwordx2 v[" #R "+14:" #R "+15], %[vo], %[sb] offset:1920 sc1\n\t" \
      "global_load_dwordx2 v[" #R ":" #R "+1], %[vml], %[sb] offset:-2560 sc1\n\t"
#define A_TRY(R, LBL) \
      "s_waitcnt vmcnt(8)\n\t" \
      "v_cndmask_b32 v20, v[" #R "], v28, %[own]\n\t" \
      "v_cndmask_b32 v21, v[" #R "], v28, %[own]\n\t" \
      "v_xor_b32 v22, %[tag], v[" #R "+1]\n\t" \
      "v_xor_b32 v23, %[tag], v[" #R "+3]\n\t" \
      "v_mov_b32_dpp v20, v21 row_ror:8 row_mask:0xf bank_mask:0xc\n\t"     \
      "v_mov_b32_dpp v21, v21 row_ror:8 row_mask:0xf bank_mask:0x3\n\t"     \
      "v_bitop3_b32 v22, v22, v[" #R "+5], %[tag] bitop3:0xf6\n\t"          \
      "v_max_f32_dpp v18, v20, v20 quad_perm:[1,0,3,2]" DPPM \
      "v_bitop3_b32 v23, v23, v[" #R "+7], %[tag] bitop3:0xf6\n\t" \
      "v_bitop3_b32 v22, v22, v[" #R "+9], %[tag] bitop3:0xf6\n\t" \
      "v_max_f32_dpp v18, v18, v18 quad_perm:[2,3,0,1]" DPPM \
      "v_bitop3_b32 v23, v23, v[" #R "+11], %[tag] bitop3:0xf6\n\t" \
      "v_bitop3_b32 v22, v22, v[" #R "+13], %[tag] bitop3:0xf6\n\t" \
      "v_max_f32_dpp v18, v18, v18 row_half_mirror" DPPM \
      "v_bitop3_b32 v23, v23, v[" #R "+15], %[tag] bitop3:0xf6\n\t" \
      "v_sub_f32 v20, v20, v18\n\t" \
      "v_exp_f32 v20, v20\n\t"                                              \
      "v_or_b32 v22, v22, v23\n\t" \
      "v_cmp_ne_u32 vcc, 0, v22\n\t" \
      "v_mul_f32 v21, v20, v21\n\t" \
      "s_cbranch_vccnz " LBL "f\n\t" \
      "v_mul_f32_dpp v16, v20, v24 row_newbcast:0" DPPM \
      "v_mul_f32_dpp v17, v20, v[" #R "+2] row_newbcast:1" DPPM \
      "v_add_f32_dpp v21, v21, v21 quad_perm:[1,0,3,2]" DPPM \
      "v_fmac_f32_dpp v16, v20, v[" #R "+4] row_newbcast:2" DPPM \
      "v_fmac_f32_dpp v17, v20, v[" #R "+6] row_newbcast:3" DPPM \
      "v_add_f32_dpp v21, v21, v21 quad_perm:[2,3,0,1]" DPPM \
      "v_fmac_f32_dpp v16, v20, v[" #R "+8] row_newbcast:4" DPPM \
      "v_fmac_f32_dpp v17, v20, v[" #R "+10] row_newbcast:5" DPPM \
      "v_add_f32_dpp v21, v21, v21 row_half_mirror" DPPM \
      "v_fmac_f32_dpp v16, v20, v[" #R "+12] row_newbcast:6" DPPM \
      "v_fmac_f32_dpp v17, v20, v[" #R "+14] row_newbcast:7" DPPM \
      "v_rcp_f32 v21, v21\n\t" \
      "v_add_f32 v16, v16, v17\n\t" \
      "v_mul_f32 v16, v16, v21\n\t" \
      "v_cvt_pk_bf16_f32 v16, v16, v16\n\t" \
      "global_store_short %[vout], v16, %[sout] sc1\n\t" \
      "global_store_dword v30, %[tag], %[sep] sc1\n\t" \
      "s_endpgm\n\t" \
      LBL ":\n\t"
  static_assert(SLOT_B == 640 && W == 16 && NSPLIT == 8, "offsets in the asm");
  asm volatile(
      "ds_read_b64 v[26:27], %[vl]\n\t"
      "ds_read2st64_b32 v[0:1], %[vrec] offset1:2\n\t"
      "ds_read2st64_b32 v[2:3], %[vrec] offset0:4 offset1:6\n\t"
      "ds_read2st64_b32 v[4:5], %[vrec] offset0:8 offset1:10\n\t"
      "ds_read2st64_b32 v[6:7], %[vrec] offset0:12 offset1:14\n\t"
      "ds_read2st64_b32 v[8:9], %[vrec] offset0:16 offset1:18\n\t"
      "ds_read2st64_b32 v[10:11], %[vrec] offset0:20 offset1:22\n\t"
      "ds_read2st64_b32 v[12:13], %[vrec] offset0:24 offset1:26\n\t"
      "ds_read2st64_b32 v[14:15], %[vrec] offset0:28 offset1:30\n\t"
      "s_cmp_eq_u32 %[s], 0\n\t"
      "s_waitcnt lgkmcnt(8)\n\t"
      "v_max_f32_dpp v28, v26, v26 quad_perm:[1,0,3,2]" DPPM
      "v_mov_b32 v25, %[tag]\n\t"
      "v_mov_b32 v29, %[tag]\n\t"
      "v_max_f32_dpp v28, v28, v28 quad_perm:[2,3,0,1]" DPPM
      "v_mov_b32 v30, 0\n\t"
      "s_nop 0\n\t"
      "v_max_f32_dpp v28, v28, v28 row_half_mirror" DPPM
      "s_nop 1\n\t"
      "v_max_f32_dpp v28, v28, v28 row_mirror" DPPM
      "v_sub_f32 v26, v26, v28\n\t"
      "v_exp_f32 v26, v26\n\t"
      "s_waitcnt lgkmcnt(0)\n\t"
      "v_mul_f32 v27, v26, v27\n\t"
      "v_mul_f32_dpp v16, v26, v0 row_newbcast:0" DPPM
      "v_mul_f32_dpp v17, v26, v1 row_newbcast:1" DPPM
      "v_add_f32_dpp v27, v27, v27 quad_perm:[1,0,3,2]" DPPM
      "v_mul_f32_dpp v18, v26, v2 row_newbcast:2" DPPM
      "v_mul_f32_dpp v19, v26, v3 row_newbcast:3" DPPM
      "v_add_f32_dpp v27, v27, v27 quad_perm:[2,3,0,1]" DPPM
      "v_fmac_f32_dpp v16, v26, v4 row_newbcast:4" DPPM
      "v_fmac_f32_dpp v17, v26, v5 row_newbcast:5" DPPM
      "v_fmac_f32_dpp v18, v26, v6 row_newbcast:6" DPPM
      "v_fmac_f32_dpp v19, v26, v7 row_newbcast:7" DPPM
      "v_add_f32_dpp v27, v27, v27 row_half_mirror" DPPM
      "v_fmac_f32_dpp v16, v26, v8 row_newbcast:8" DPPM
      "v_fmac_f32_dpp v17, v26, v9 row_newbcast:9" DPPM
      "v_fmac_f32_dpp v18, v26, v10 row_newbcast:10" DPPM
      "v_fmac_f32_dpp v19, v26, v11 row_newbcast:11" DPPM
      "v_add_f32_dpp v27, v27, v27 row_mirror" DPPM
      "v_fmac_f32_dpp v16, v26, v12 row_newbcast:12" DPPM
      "v_fmac_f32_dpp v17, v26, v13 row_newbcast:13" DPPM
      "v_fmac_f32_dpp v18, v26, v14 row_newbcast:14" DPPM
      "v_fmac_f32_dpp v19, v26, v15 row_newbcast:15" DPPM
      "v_pk_add_f32 v[16:17], v[16:17], v[18:19]\n\t"
      "v_cndmask_b32 v28, v28, v27, %[hi8]\n\t"
      "v_add_f32 v24, v16, v17\n\t"
      "s_cbranch_scc1 2f\n\t"
      "global_store_dwordx2 %[vme], v[24:25], %[sb] offset:-2560 sc1\n\t"
      "global_store_dwordx2 %[vmy], v[28:29], %[sb] offset:-2560 sc1\n\t"
      "s_endpgm\n\t"
      "2:\n\t"
      "s_mov_b32 s44, 0x100000\n\t"
      "s_sleep " SWEEP_D "\n\t"
      "4:\n\t"
      "s_memrealtime s[42:43]\n\t"
      "s_waitcnt lgkmcnt(0)\n\t"
      "s_sub_u32 s42, s42, %[t0]\n\t"
      "s_cmpk_lt_u32 s42, %c[tx]\n\t"
      "s_cbranch_scc1 4b\n\t"
      A_SWEEP(0)
      "s_sleep " SWEEP_P "\n\t"
      A_SWEEP(32)
      "1:\n\t"
      A_TRY(0, "5") A_SWEEP(0)
      A_TRY(32, "6") A_SWEEP(32)
      "s_sub_u32 s44, s44, 1\n\t"
      "s_cbranch_scc0 1b\n\t"
      "s_endpgm"
      :
      : [vo] "v"(vo), [vme] "v"(vme), [vml] "v"(vml), [vmy] "v"(vmy), [vrec] "v"(vrec), [vl] "v"(vl), [vout] "v"(vout), [tag] "v"(tag),
        [sb] "s"(sb), [own] "s"(own), [hi8] "s"(hi8), [sout] "s"(out), [sep] "s"(hw), [s] "s"(s),
        [t0] "s"(t0), [tx] "i"((2 * W - 1) * PACE_NS / 10 + SWEEP_T)
      : "memory", "vcc", "scc", "s42", "s43", "s44", "v0", "v1", "v2", "v3", "v4", "v5", "v6", "v7", "v8", "v9", "v10", "v11", "v12", "v13",
        "v14", "v15", "v16", "v17", "v18", "v19", "v20", "v21", "v22", "v23", "v24", "v25", "v26", "v27", "v28", "v29", "v30",
        "v32", "v33", "v34", "v35", "v36", "v37", "v38", "v39", "v40", "v41", "v42", "v43", "v44", "v45", "v46", "v47");
}

extern "C" void kernel_launch(void* const* d_in, const int* in_sizes, int n_in, void* d_out, int out_size, void* d_ws,
                              size_t ws_size, hipStream_t stream) {
  attn_decode<<<H * NSPLIT, W * 64, 0, stream>>>(d_in[0], d_in[1], d_in[2], d_out, d_ws);
}
```

```cpp
#include <hip/hip_runtime.h>
#include <hip/hip_bf16.h>

constexpr int PACE_NS = 270;
constexpr int SWEEP_T = 114;
#define SWEEP_D "3"
#define SWEEP_P "4"

constexpr int H = 32, D = 128, NSPLIT = 8, W = 16;

using bf16x2 = __attribute__((ext_vector_type(2))) __bf16;
using bf16x8 = __attribute__((ext_vector_type(8))) __bf16;
using u32x4 = __attribute__((ext_vector_type(4))) unsigned;
using f32x4 = __attribute__((ext_vector_type(4))) float;
using f32x2 = __attribute__((ext_vector_type(2))) float;
using u64 = unsigned long long;
using u64x4 = __attribute__((ext_vector_type(4))) u64;
typedef __attribute__((address_space(1))) unsigned char gu8;
typedef __attribute__((address_space(1))) unsigned gu32;
typedef __attribute__((address_space(1))) u64 gu64;
typedef __attribute__((address_space(1))) u32x4 gu32x4;
#define RLX_AGENT __ATOMIC_RELAXED, __HIP_MEMORY_SCOPE_AGENT
#define SB() __builtin_amdgcn_sched_barrier(0)

__device__ __forceinline__ float dot2(unsigned k, unsigned q, float acc) {
  return __builtin_amdgcn_fdot2_f32_bf16(__builtin_bit_cast(bf16x2, k), __builtin_bit_cast(bf16x2, q), acc, false);
}
__device__ __forceinline__ unsigned cvtpk(float lo, float hi) {
  bf16x2 r = {(__bf16)lo, (__bf16)hi};
  return __builtin_bit_cast(unsigned, r);
}
template <int CTRL> __device__ __forceinline__ float dpp(float x) {
  return __builtin_bit_cast(float, __builtin_amdgcn_mov_dpp(__builtin_bit_cast(int, x), CTRL, 0xf, 0xf, true));
}
__device__ __forceinline__ float row_sum(float x) {
  x += dpp<0xB1>(x); x += dpp<0x4E>(x); x += dpp<0x141>(x); x += dpp<0x140>(x); return x;
}
__device__ __forceinline__ float xrow_max(float x) {
  auto s = __builtin_amdgcn_permlane16_swap(__float_as_uint(x), __float_as_uint(x), false, false);
  x = fmaxf(__uint_as_float(s[0]), __uint_as_float(s[1]));
  auto t = __builtin_amdgcn_permlane32_swap(__float_as_uint(x), __float_as_uint(x), false, false);
  return fmaxf(__uint_as_float(t[0]), __uint_as_float(t[1]));
}
__device__ __forceinline__ float xrow_sum(float x) {
  auto s = __builtin_amdgcn_permlane16_swap(__float_as_uint(x), __float_as_uint(x), false, false);
  x = __uint_as_float(s[0]) + __uint_as_float(s[1]);
  auto t = __builtin_amdgcn_permlane32_swap(__float_as_uint(x), __float_as_uint(x), false, false);
  return __uint_as_float(t[0]) + __uint_as_float(t[1]);
}
__device__ __forceinline__ unsigned now() { return (unsigned)__builtin_amdgcn_s_memrealtime(); }

constexpr unsigned HEAD_B = 16384, SLOTS_B = 1024, SLOT_B = 640;

__global__ void __launch_bounds__(W * 64, W / 4)
attn_decode(const void* q_, const void* k_, const void* v_, void* out_, void* ws_) {
  __shared__ __attribute__((aligned(16))) float lds[W * D + 2 * W + 1];
  u64x4 a03;
  u64 a4;
  asm volatile("s_load_dwordx8 %0, %2, 0x0\n\ts_load_dwordx2 %1, %2, 0x20"
               : "=&s"(a03), "=&s"(a4) : "s"(__builtin_amdgcn_kernarg_segment_ptr()));
  SB();
  const int b = blockIdx.x;
  const int h = (b & 7) * 4 + ((b >> 3) & 3), s = b >> 5;
  const int tid = threadIdx.x, lane = tid & 63, wave = __builtin_amdgcn_readfirstlane(tid >> 6);
  const int c = lane & 15;
  constexpr float C2 = 0.088388347648318440f * 1.4426950408889634f;

  unsigned so = ((unsigned)h << 20) + (((wave + h) & 15) << 16) + (((s + wave) & 7) << 13) + 4096 + lane * 16;
  unsigned vq = h * 256 + c * 16, vep = h * HEAD_B;
  asm volatile("" : "+v"(so), "+v"(vq), "+v"(vep));
  SB();
  u64 t64;
  asm volatile("s_waitcnt lgkmcnt(0)\n\ts_memrealtime s[40:41]" : "+s"(a03), "+s"(a4), "=&{s[40:41]}"(t64));
  gu8 *const q = (gu8*)a03[0], *const k = (gu8*)a03[1], *const v = (gu8*)a03[2], *const out = (gu8*)a03[3], *const ws = (gu8*)a4;
  const u32x4 qf = *(const gu32x4*)(q + vq);
  const unsigned e0 = __hip_atomic_load((gu32*)(ws + vep), RLX_AGENT);
  SB();
  asm volatile("s_cmp_eq_u32 %[g], 0\n\t"
               "s_cbranch_scc1 2f\n\t"
               "1:\n\t"
               "s_sleep 1\n\t"
               "s_memrealtime s[42:43]\n\t"
               "s_waitcnt lgkmcnt(0)\n\t"
               "s_sub_u32 s42, s42, s40\n\t"
               "s_cmp_lt_u32 s42, %[g]\n\t"
               "s_cbranch_scc1 1b\n\t"
               "2:"
               : "+{s[40:41]}"(t64) : [g] "s"((wave * (PACE_NS * 1024 / 10)) >> 10) : "scc", "s42", "s43");
#define LD(dst, p) _Pragma("unroll") for (int i = 0; i < 8; ++i) { \
    dst[i] = __builtin_nontemporal_load((const gu32x4*)(p + (size_t)so + (i * 1024 - 4096))); SB(); }
  u32x4 kr[8], vr[8], pf[8];
  SB();
  LD(kr, k);
  asm volatile("s_waitcnt lgkmcnt(0)" : "+{s[40:41]}"(t64));
  if (wave == 0) lds[W * D + 2 * W] = __uint_as_float((unsigned)t64);
  gu8* hw = ws + h * HEAD_B;
  unsigned tag = e0 + 0x9E3779B9u;
  gu64* mine = (gu64*)(hw + SLOTS_B + (((tid >> 7) & 1) * NSPLIT + s) * SLOT_B) + (tid & 127);
  const bool ko = tid < 256 && (tid & 127) < 66;
  if (ko) __hip_atomic_store(mine, (u64)e0 << 32, RLX_AGENT);
  SB();
  if (wave >= W / 2) asm volatile("s_barrier" ::: "memory");
  SB();

  float x[8];
#pragma unroll
  for (int i = 0; i < 8; ++i) {
    float d = dot2(kr[i][0], qf[0], 0.f);
    d = dot2(kr[i][1], qf[1], d); d = dot2(kr[i][2], qf[2], d); d = dot2(kr[i][3], qf[3], d);
    x[i] = row_sum(d) * C2;
  }
  float m = x[0], l = 0.f;
#pragma unroll
  for (int i = 1; i < 8; ++i) m = fmaxf(m, x[i]);
  m = xrow_max(m);
  const unsigned mlo = c < 8 ? 0u : 0xffff0000u;
  const unsigned half = (c & 1) ? 0xffff0000u : 0x0000ffffu;
  const int dsel = (c & 7) >> 1;
  const unsigned mk0 = dsel == 0 ? half : 0u, mk1 = dsel == 1 ? half : 0u, mk2 = dsel == 2 ? half : 0u, mk3 = dsel == 3 ? half : 0u;
#pragma unroll
  for (int i = 0; i < 8; ++i) {
    const float p = __builtin_amdgcn_exp2f(x[i] - m);
    l += p;
    const unsigned hi = cvtpk(p, p);
    const float r = p - __uint_as_float(hi & mlo);
    const unsigned pk = cvtpk(r, r);
    pf[i] = u32x4{pk & mk0, pk & mk1, pk & mk2, pk & mk3};
    asm volatile("" : "+v"(pf[i]));
  }
  u64 pre = 0;
  if (ko) pre = __hip_atomic_load(mine, RLX_AGENT);
  if (wave < W / 2) asm volatile("s_waitcnt lgkmcnt(0)\n\ts_barrier" ::: "memory");
  const unsigned t0 = __builtin_amdgcn_readfirstlane(__float_as_uint(lds[W * D + 2 * W]));
  SB();
  {
    const unsigned g = (W + wave) * PACE_NS / 10;
    while (now() - t0 < g - 36) __builtin_amdgcn_s_sleep(8);
    while (now() - t0 < g) __builtin_amdgcn_s_sleep(1);
    SB();
  }
  LD(vr, v);
  l = xrow_sum(l);
  *reinterpret_cast<f32x2*>(lds + W * D + 2 * wave) = f32x2{m, l};
  const gu8* sb = hw + SLOTS_B + (wave & 1) * (NSPLIT * SLOT_B) + 4 * SLOT_B;
  unsigned vo = lane * 8, vme = s * SLOT_B + lane * 8;
  unsigned vml = ((c & 7) == 0 ? 1 : (c & 7)) * SLOT_B + 512 + (c >> 3) * 8;
  unsigned vmy = s * SLOT_B + 512 + (c >> 3) * 8;
  const unsigned lbase = (unsigned)(size_t)(__attribute__((address_space(3))) float*)lds;
  unsigned vrec = lbase + ((wave & 1) * 64 + lane) * 4, vl = lbase + (W * D + 2 * c) * 4, vout = h * 256 + (tid & 127) * 2;
  const u64 own = __ballot((c & 7) == 0), hi8 = __ballot(c >= 8);
  unsigned vw = lbase + (wave * D + 8 * c + 4 * ((lane >> 4) & 1) + 2 * (lane >> 5)) * 4;
  asm volatile("" : "+v"(vo), "+v"(vme), "+v"(vml), "+v"(vmy), "+v"(vrec), "+v"(vl), "+v"(vout), "+v"(vw), "+v"(tag)
               : "s"(out), "s"(sb), "s"(own), "s"(hi8));
  SB();
  f32x4 acc = {0.f, 0.f, 0.f, 0.f};
#pragma unroll
  for (int i = 0; i < 8; ++i)
    acc = __builtin_amdgcn_mfma_f32_16x16x32_bf16(__builtin_bit_cast(bf16x8, pf[i]), __builtin_bit_cast(bf16x8, vr[i]), acc, 0, 0, 0);
  auto s02 = __builtin_amdgcn_permlane32_swap(__float_as_uint(acc[0]), __float_as_uint(acc[2]), false, false);
  auto s13 = __builtin_amdgcn_permlane32_swap(__float_as_uint(acc[1]), __float_as_uint(acc[3]), false, false);
  const f32x2 o2 = {__uint_as_float(s02[0]) + __uint_as_float(s02[1]), __uint_as_float(s13[0]) + __uint_as_float(s13[1])};
  *(__attribute__((address_space(3))) f32x2*)(size_t)vw = o2;
  asm volatile("" :: "v"(pre));
  asm volatile("s_waitcnt vmcnt(0) lgkmcnt(0)\n\ts_barrier" ::: "memory");
  if (wave >= 2) return;

#define DPPM " row_mask:0xf bank_mask:0xf\n\t"
#define A_SWEEP(R) \
      "global_load_dwordx2 v[" #R "+2:" #R "+3], %[vo], %[sb] offset:-1920 sc0 sc1\n\t" \
      "global_load_dwordx2 v[" #R "+4:" #R "+5], %[vo], %[sb] offset:-1280 sc0 sc1\n\t" \
      "global_load_dwordx2 v[" #R "+6:" #R "+7], %[vo], %[sb] offset:-640 sc0 sc1\n\t" \
      "global_load_dwordx2 v[" #R "+8:" #R "+9], %[vo], %[sb] sc0 sc1\n\t" \
      "global_load_dwordx2 v[" #R "+10:" #R "+11], %[vo], %[sb] offset:640 sc0 sc1\n\t" \
      "global_load_dwordx2 v[" #R "+12:" #R "+13], %[vo], %[sb] offset:1280 sc0 sc1\n\t" \
      "global_load_dwordx2 v[" #R "+14:" #R "+15], %[vo], %[sb] offset:1920 sc0 sc1\n\t" \
      "global_load_dwordx2 v[" #R ":" #R "+1], %[vml], %[sb] offset:-2560 sc0 sc1\n\t"
#define A_TRY(R, LBL) \
      "s_waitcnt vmcnt(8)\n\t" \
      "v_cndmask_b32 v20, v[" #R "], v28, %[own]\n\t" \
      "v_cndmask_b32 v21, v[" #R "], v28, %[own]\n\t" \
      "v_xor_b32 v22, %[tag], v[" #R "+1]\n\t" \
      "v_xor_b32 v23, %[tag], v[" #R "+3]\n\t" \
      "v_mov_b32_dpp v20, v21 row_ror:8 row_mask:0xf bank_mask:0xc\n\t"     \
      "v_mov_b32_dpp v21, v21 row_ror:8 row_mask:0xf bank_mask:0x3\n\t"     \
      "v_bitop3_b32 v22, v22, v[" #R "+5], %[tag] bitop3:0xf6\n\t"          \
      "v_max_f32_dpp v18, v20, v20 quad_perm:[1,0,3,2]" DPPM \
      "v_bitop3_b32 v23, v23, v[" #R "+7], %[tag] bitop3:0xf6\n\t" \
      "v_bitop3_b32 v22, v22, v[" #R "+9], %[tag] bitop3:0xf6\n\t" \
      "v_max_f32_dpp v18, v18, v18 quad_perm:[2,3,0,1]" DPPM \
      "v_bitop3_b32 v23, v23, v[" #R "+11], %[tag] bitop3:0xf6\n\t" \
      "v_bitop3_b32 v22, v22, v[" #R "+13], %[tag] bitop3:0xf6\n\t" \
      "v_max_f32_dpp v18, v18, v18 row_half_mirror" DPPM \
      "v_bitop3_b32 v23, v23, v[" #R "+15], %[tag] bitop3:0xf6\n\t" \
      "v_sub_f32 v20, v20, v18\n\t" \
      "v_exp_f32 v20, v20\n\t"                                              \
      "v_or_b32 v22, v22, v23\n\t" \
      "v_cmp_ne_u32 vcc, 0, v22\n\t" \
      "v_mul_f32 v21, v20, v21\n\t" \
      "s_cbranch_vccnz " LBL "f\n\t" \
      "v_mul_f32_dpp v16, v20, v24 row_newbcast:0" DPPM \
      "v_mul_f32_dpp v17, v20, v[" #R "+2] row_newbcast:1" DPPM \
      "v_add_f32_dpp v21, v21, v21 quad_perm:[1,0,3,2]" DPPM \
      "v_fmac_f32_dpp v16, v20, v[" #R "+4] row_newbcast:2" DPPM \
      "v_fmac_f32_dpp v17, v20, v[" #R "+6] row_newbcast:3" DPPM \
      "v_add_f32_dpp v21, v21, v21 quad_perm:[2,3,0,1]" DPPM \
      "v_fmac_f32_dpp v16, v20, v[" #R "+8] row_newbcast:4" DPPM \
      "v_fmac_f32_dpp v17, v20, v[" #R "+10] row_newbcast:5" DPPM \
      "v_add_f32_dpp v21, v21, v21 row_half_mirror" DPPM \
      "v_fmac_f32_dpp v16, v20, v[" #R "+12] row_newbcast:6" DPPM \
      "v_fmac_f32_dpp v17, v20, v[" #R "+14] row_newbcast:7" DPPM \
      "v_rcp_f32 v21, v21\n\t" \
      "v_add_f32 v16, v16, v17\n\t" \
      "v_mul_f32 v16, v16, v21\n\t" \
      "v_cvt_pk_bf16_f32 v16, v16, v16\n\t" \
      "global_store_short %[vout], v16, %[sout] sc1\n\t" \
      "global_store_dword v30, %[tag], %[sep] sc1\n\t" \
      "s_branch 9f\n\t" \
      LBL ":\n\t"
  static_assert(SLOT_B == 640 && W == 16 && NSPLIT == 8, "offsets in the asm");
  asm volatile(
      "ds_read_b64 v[26:27], %[vl]\n\t"
      "ds_read2st64_b32 v[0:1], %[vrec] offset1:2\n\t"
      "ds_read2st64_b32 v[2:3], %[vrec] offset0:4 offset1:6\n\t"
      "ds_read2st64_b32 v[4:5], %[vrec] offset0:8 offset1:10\n\t"
      "ds_read2st64_b32 v[6:7], %[vrec] offset0:12 offset1:14\n\t"
      "ds_read2st64_b32 v[8:9], %[vrec] offset0:16 offset1:18\n\t"
      "ds_read2st64_b32 v[10:11], %[vrec] offset0:20 offset1:22\n\t"
      "ds_read2st64_b32 v[12:13], %[vrec] offset0:24 offset1:26\n\t"
      "ds_read2st64_b32 v[14:15], %[vrec] offset0:28 offset1:30\n\t"
      "s_cmp_eq_u32 %[s], 0\n\t"
      "s_waitcnt lgkmcnt(8)\n\t"
      "v_max_f32_dpp v28, v26, v26 quad_perm:[1,0,3,2]" DPPM
      "v_mov_b32 v25, %[tag]\n\t"
      "v_mov_b32 v29, %[tag]\n\t"
      "v_max_f32_dpp v28, v28, v28 quad_perm:[2,3,0,1]" DPPM
      "v_mov_b32 v30, 0\n\t"
      "s_nop 0\n\t"
      "v_max_f32_dpp v28, v28, v28 row_half_mirror" DPPM
      "s_nop 1\n\t"
      "v_max_f32_dpp v28, v28, v28 row_mirror" DPPM
      "v_sub_f32 v26, v26, v28\n\t"
      "v_exp_f32 v26, v26\n\t"
      "s_waitcnt lgkmcnt(0)\n\t"
      "v_mul_f32 v27, v26, v27\n\t"
      "v_mul_f32_dpp v16, v26, v0 row_newbcast:0" DPPM
      "v_mul_f32_dpp v17, v26, v1 row_newbcast:1" DPPM
      "v_add_f32_dpp v27, v27, v27 quad_perm:[1,0,3,2]" DPPM
      "v_mul_f32_dpp v18, v26, v2 row_newbcast:2" DPPM
      "v_mul_f32_dpp v19, v26, v3 row_newbcast:3" DPPM
      "v_add_f32_dpp v27, v27, v27 quad_perm:[2,3,0,1]" DPPM
      "v_fmac_f32_dpp v16, v26, v4 row_newbcast:4" DPPM
      "v_fmac_f32_dpp v17, v26, v5 row_newbcast:5" DPPM
      "v_fmac_f32_dpp v18, v26, v6 row_newbcast:6" DPPM
      "v_fmac_f32_dpp v19, v26, v7 row_newbcast:7" DPPM
      "v_add_f32_dpp v27, v27, v27 row_half_mirror" DPPM
      "v_fmac_f32_dpp v16, v26, v8 row_newbcast:8" DPPM
      "v_fmac_f32_dpp v17, v26, v9 row_newbcast:9" DPPM
      "v_fmac_f32_dpp v18, v26, v10 row_newbcast:10" DPPM
      "v_fmac_f32_dpp v19, v26, v11 row_newbcast:11" DPPM
      "v_add_f32_dpp v27, v27, v27 row_mirror" DPPM
      "v_fmac_f32_dpp v16, v26, v12 row_newbcast:12" DPPM
      "v_fmac_f32_dpp v17, v26, v13 row_newbcast:13" DPPM
      "v_fmac_f32_dpp v18, v26, v14 row_newbcast:14" DPPM
      "v_fmac_f32_dpp v19, v26, v15 row_newbcast:15" DPPM
      "v_pk_add_f32 v[16:17], v[16:17], v[18:19]\n\t"
      "v_cndmask_b32 v28, v28, v27, %[hi8]\n\t"
      "v_add_f32 v24, v16, v17\n\t"
      "s_cbranch_scc1 2f\n\t"
      "global_store_dwordx2 %[vme], v[24:25], %[sb] offset:-2560 sc0 sc1\n\t"
      "global_store_dwordx2 %[vmy], v[28:29], %[sb] offset:-2560 sc0 sc1\n\t"
      "s_endpgm\n\t"
      "2:\n\t"
      "s_mov_b32 s44, 0x100000\n\t"
      "s_sleep " SWEEP_D "\n\t"
      "4:\n\t"
      "s_memrealtime s[42:43]\n\t"
      "s_waitcnt lgkmcnt(0)\n\t"
      "s_sub_u32 s42, s42, %[t0]\n\t"
      "s_cmpk_lt_u32 s42, %c[tx]\n\t"
      "s_cbranch_scc1 4b\n\t"
      A_SWEEP(0)
      "s_sleep " SWEEP_P "\n\t"
      A_SWEEP(32)
      "1:\n\t"
      A_TRY(0, "5") A_SWEEP(0)
      A_TRY(32, "6") A_SWEEP(32)
      "s_sub_u32 s44, s44, 1\n\t"
      "s_cbranch_scc0 1b\n\t"
      "9:"
      :
      : [vo] "v"(vo), [vme] "v"(vme), [vml] "v"(vml), [vmy] "v"(vmy), [vrec] "v"(vrec), [vl] "v"(vl), [vout] "v"(vout), [tag] "v"(tag),
        [sb] "s"(sb), [own] "s"(own), [hi8] "s"(hi8), [sout] "s"(out), [sep] "s"(hw), [s] "s"(s),
        [t0] "s"(t0), [tx] "i"((2 * W - 1) * PACE_NS / 10 + SWEEP_T)
      : "memory", "vcc", "scc", "s42", "s43", "s44", "v0", "v1", "v2", "v3", "v4", "v5", "v6", "v7", "v8", "v9", "v10", "v11", "v12", "v13",
        "v14", "v15", "v16", "v17", "v18", "v19", "v20", "v21", "v22", "v23", "v24", "v25", "v26", "v27", "v28", "v29", "v30",
        "v32", "v33", "v34", "v35", "v36", "v37", "v38", "v39", "v40", "v41", "v42", "v43", "v44", "v45", "v46", "v47");
  { const unsigned n0 = now(); int sk = ((int)(n0 - t0) - 960) * 10; if (sk > 5000) sk = 5000; while ((int)(now() - n0) < sk) __builtin_amdgcn_s_sleep(2); }
}

extern "C" void kernel_launch(void* const* d_in, const int* in_sizes, int n_in, void* d_out, int out_size, void* d_ws,
                              size_t ws_size, hipStream_t stream) {
  attn_decode<<<H * NSPLIT, W * 64, 0, stream>>>(d_in[0], d_in[1], d_in[2], d_out, d_ws);
}
```

```cpp
#include <hip/hip_runtime.h>
#include <hip/hip_bf16.h>

constexpr int PACE_NS = 272;
constexpr int SWEEP_T = 113;
#define SWEEP_D "3"
#define SWEEP_P "4"

constexpr int H = 32, D = 128, NSPLIT = 8, W = 16;

using bf16x2 = __attribute__((ext_vector_type(2))) __bf16;
using bf16x8 = __attribute__((ext_vector_type(8))) __bf16;
using u32x4 = __attribute__((ext_vector_type(4))) unsigned;
using f32x4 = __attribute__((ext_vector_type(4))) float;
using f32x2 = __attribute__((ext_vector_type(2))) float;
using u64 = unsigned long long;
using u64x4 = __attribute__((ext_vector_type(4))) u64;
typedef __attribute__((address_space(1))) unsigned char gu8;
typedef __attribute__((address_space(1))) unsigned gu32;
typedef __attribute__((address_space(1))) u64 gu64;
typedef __attribute__((address_space(1))) u32x4 gu32x4;
#define RLX_AGENT __ATOMIC_RELAXED, __HIP_MEMORY_SCOPE_AGENT
#define SB() __builtin_amdgcn_sched_barrier(0)

__device__ __forceinline__ float dot2(unsigned k, unsigned q, float acc) {
  return __builtin_amdgcn_fdot2_f32_bf16(__builtin_bit_cast(bf16x2, k), __builtin_bit_cast(bf16x2, q), acc, false);
}
__device__ __forceinline__ unsigned cvtpk(float lo, float hi) {
  bf16x2 r = {(__bf16)lo, (__bf16)hi};
  return __builtin_bit_cast(unsigned, r);
}
template <int CTRL> __device__ __forceinline__ float dpp(float x) {
  return __builtin_bit_cast(float, __builtin_amdgcn_mov_dpp(__builtin_bit_cast(int, x), CTRL, 0xf, 0xf, true));
}
__device__ __forceinline__ float row_sum(float x) {
  x += dpp<0xB1>(x); x += dpp<0x4E>(x); x += dpp<0x141>(x); x += dpp<0x140>(x); return x;
}
__device__ __forceinline__ float xrow_max(float x) {
  auto s = __builtin_amdgcn_permlane16_swap(__float_as_uint(x), __float_as_uint(x), false, false);
  x = fmaxf(__uint_as_float(s[0]), __uint_as_float(s[1]));
  auto t = __builtin_amdgcn_permlane32_swap(__float_as_uint(x), __float_as_uint(x), false, false);
  return fmaxf(__uint_as_float(t[0]), __uint_as_float(t[1]));
}
__device__ __forceinline__ float xrow_sum(float x) {
  auto s = __builtin_amdgcn_permlane16_swap(__float_as_uint(x), __float_as_uint(x), false, false);
  x = __uint_as_float(s[0]) + __uint_as_float(s[1]);
  auto t = __builtin_amdgcn_permlane32_swap(__float_as_uint(x), __float_as_uint(x), false, false);
  return __uint_as_float(t[0]) + __uint_as_float(t[1]);
}
__device__ __forceinline__ unsigned now() { return (unsigned)__builtin_amdgcn_s_memrealtime(); }

constexpr unsigned HEAD_B = 16384, SLOTS_B = 1024, SLOT_B = 640;

__global__ void __launch_bounds__(W * 64, W / 4)
attn_decode(const void* q_, const void* k_, const void* v_, void* out_, void* ws_) {
  __shared__ __attribute__((aligned(16))) float lds[W * D + 2 * W + 1];
  u64x4 a03;
  u64 a4;
  asm volatile("s_load_dwordx8 %0, %2, 0x0\n\ts_load_dwordx2 %1, %2, 0x20"
               : "=&s"(a03), "=&s"(a4) : "s"(__builtin_amdgcn_kernarg_segment_ptr()));
  SB();
  const int b = blockIdx.x;
  const int h = (b & 7) * 4 + ((b >> 3) & 3), s = b >> 5;
  const int tid = threadIdx.x, lane = tid & 63, wave = __builtin_amdgcn_readfirstlane(tid >> 6);
  const int c = lane & 15;
  constexpr float C2 = 0.088388347648318440f * 1.4426950408889634f;

  unsigned so = ((unsigned)h << 20) + (((wave + h) & 15) << 16) + (((s + wave) & 7) << 13) + 4096 + lane * 16;
  unsigned vq = h * 256 + c * 16, vep = h * HEAD_B;
  asm volatile("" : "+v"(so), "+v"(vq), "+v"(vep));
  SB();
  u64 t64;
  asm volatile("s_waitcnt lgkmcnt(0)\n\ts_memrealtime s[40:41]" : "+s"(a03), "+s"(a4), "=&{s[40:41]}"(t64));
  gu8 *const q = (gu8*)a03[0], *const k = (gu8*)a03[1], *const v = (gu8*)a03[2], *const out = (gu8*)a03[3], *const ws = (gu8*)a4;
  const u32x4 qf = *(const gu32x4*)(q + vq);
  const unsigned e0 = __hip_atomic_load((gu32*)(ws + vep), RLX_AGENT);
  SB();
  asm volatile("s_cmp_eq_u32 %[g], 0\n\t"
               "s_cbranch_scc1 2f\n\t"
               "1:\n\t"
               "s_sleep 1\n\t"
               "s_memrealtime s[42:43]\n\t"
               "s_waitcnt lgkmcnt(0)\n\t"
               "s_sub_u32 s42, s42, s40\n\t"
               "s_cmp_lt_u32 s42, %[g]\n\t"
               "s_cbranch_scc1 1b\n\t"
               "2:"
               : "+{s[40:41]}"(t64) : [g] "s"((wave * (PACE_NS * 1024 / 10)) >> 10) : "scc", "s42", "s43");
#define LD(dst, p) _Pragma("unroll") for (int i = 0; i < 8; ++i) { \
    dst[i] = __builtin_nontemporal_load((const gu32x4*)(p + (size_t)so + (i * 1024 - 4096))); SB(); }
  u32x4 kr[8], vr[8], pf[8];
  SB();
  LD(kr, k);
  asm volatile("s_waitcnt lgkmcnt(0)" : "+{s[40:41]}"(t64));
  if (wave == 0) lds[W * D + 2 * W] = __uint_as_float((unsigned)t64);
  gu8* hw = ws + h * HEAD_B;
  unsigned tag = e0 + 0x9E3779B9u;
  gu64* mine = (gu64*)(hw + SLOTS_B + (((tid >> 7) & 1) * NSPLIT + s) * SLOT_B) + (tid & 127);
  const bool ko = tid < 256 && (tid & 127) < 66;
  if (ko) __hip_atomic_store(mine, (u64)e0 << 32, RLX_AGENT);
  SB();
  if (wave >= W / 2) asm volatile("s_barrier" ::: "memory");
  SB();

  float x[8];
#pragma unroll
  for (int i = 0; i < 8; ++i) {
    float d = dot2(kr[i][0], qf[0], 0.f);
    d = dot2(kr[i][1], qf[1], d); d = dot2(kr[i][2], qf[2], d); d = dot2(kr[i][3], qf[3], d);
    x[i] = row_sum(d) * C2;
  }
  float m = x[0], l = 0.f;
#pragma unroll
  for (int i = 1; i < 8; ++i) m = fmaxf(m, x[i]);
  m = xrow_max(m);
  const unsigned mlo = c < 8 ? 0u : 0xffff0000u;
  const unsigned half = (c & 1) ? 0xffff0000u : 0x0000ffffu;
  const int dsel = (c & 7) >> 1;
  const unsigned mk0 = dsel == 0 ? half : 0u, mk1 = dsel == 1 ? half : 0u, mk2 = dsel == 2 ? half : 0u, mk3 = dsel == 3 ? half : 0u;
#pragma unroll
  for (int i = 0; i < 8; ++i) {
    const float p = __builtin_amdgcn_exp2f(x[i] - m);
    l += p;
    const unsigned hi = cvtpk(p, p);
    const float r = p - __uint_as_float(hi & mlo);
    const unsigned pk = cvtpk(r, r);
    pf[i] = u32x4{pk & mk0, pk & mk1, pk & mk2, pk & mk3};
    asm volatile("" : "+v"(pf[i]));
  }
  u64 pre = 0;
  if (ko) pre = __hip_atomic_load(mine, RLX_AGENT);
  if (wave < W / 2) asm volatile("s_waitcnt lgkmcnt(0)\n\ts_barrier" ::: "memory");
  const unsigned t0 = __builtin_amdgcn_readfirstlane(__float_as_uint(lds[W * D + 2 * W]));
  SB();
  {
    const unsigned g = (W + wave) * PACE_NS / 10;
    while (now() - t0 < g - 36) __builtin_amdgcn_s_sleep(8);
    while (now() - t0 < g) __builtin_amdgcn_s_sleep(1);
    SB();
  }
  LD(vr, v);
  l = xrow_sum(l);
  *reinterpret_cast<f32x2*>(lds + W * D + 2 * wave) = f32x2{m, l};
  const gu8* sb = hw + SLOTS_B + (wave & 1) * (NSPLIT * SLOT_B) + 4 * SLOT_B;
  unsigned vo = lane * 8, vme = s * SLOT_B + lane * 8;
  unsigned vml = ((c & 7) == 0 ? 1 : (c & 7)) * SLOT_B + 512 + (c >> 3) * 8;
  unsigned vmy = s * SLOT_B + 512 + (c >> 3) * 8;
  const unsigned lbase = (unsigned)(size_t)(__attribute__((address_space(3))) float*)lds;
  unsigned vrec = lbase + ((wave & 1) * 64 + lane) * 4, vl = lbase + (W * D + 2 * c) * 4, vout = h * 256 + (tid & 127) * 2;
  const u64 own = __ballot((c & 7) == 0), hi8 = __ballot(c >= 8);
  unsigned vw = lbase + (wave * D + 8 * c + 4 * ((lane >> 4) & 1) + 2 * (lane >> 5)) * 4;
  asm volatile("" : "+v"(vo), "+v"(vme), "+v"(vml), "+v"(vmy), "+v"(vrec), "+v"(vl), "+v"(vout), "+v"(vw), "+v"(tag)
               : "s"(out), "s"(sb), "s"(own), "s"(hi8));
  SB();
  f32x4 acc = {0.f, 0.f, 0.f, 0.f};
#pragma unroll
  for (int i = 0; i < 8; ++i)
    acc = __builtin_amdgcn_mfma_f32_16x16x32_bf16(__builtin_bit_cast(bf16x8, pf[i]), __builtin_bit_cast(bf16x8, vr[i]), acc, 0, 0, 0);
  auto s02 = __builtin_amdgcn_permlane32_swap(__float_as_uint(acc[0]), __float_as_uint(acc[2]), false, false);
  auto s13 = __builtin_amdgcn_permlane32_swap(__float_as_uint(acc[1]), __float_as_uint(acc[3]), false, false);
  const f32x2 o2 = {__uint_as_float(s02[0]) + __uint_as_float(s02[1]), __uint_as_float(s13[0]) + __uint_as_float(s13[1])};
  *(__attribute__((address_space(3))) f32x2*)(size_t)vw = o2;
  asm volatile("" :: "v"(pre));
  asm volatile("s_waitcnt vmcnt(0) lgkmcnt(0)\n\ts_barrier" ::: "memory");
  if (wave >= 2) return;

#define DPPM " row_mask:0xf bank_mask:0xf\n\t"
#define A_SWEEP(R) \
      "global_load_dwordx2 v[" #R "+2:" #R "+3], %[vo], %[sb] offset:-1920 sc1\n\t" \
      "global_load_dwordx2 v[" #R "+4:" #R "+5], %[vo], %[sb] offset:-1280 sc1\n\t" \
      "global_load_dwordx2 v[" #R "+6:" #R "+7], %[vo], %[sb] offset:-640 sc1\n\t" \
      "global_load_dwordx2 v[" #R "+8:" #R "+9], %[vo], %[sb] sc1\n\t" \
      "global_load_dwordx2 v[" #R "+10:" #R "+11], %[vo], %[sb] offset:640 sc1\n\t" \
      "global_load_dwordx2 v[" #R "+12:" #R "+13], %[vo], %[sb] offset:1280 sc1\n\t" \
      "global_load_dwordx2 v[" #R "+14:" #R "+15], %[vo], %[sb] offset:1920 sc1\n\t" \
      "global_load_dwordx2 v[" #R ":" #R "+1], %[vml], %[sb] offset:-2560 sc1\n\t"
#define A_TRY(R, LBL) \
      "s_waitcnt vmcnt(8)\n\t" \
      "v_cndmask_b32 v20, v[" #R "], v28, %[own]\n\t" \
      "v_cndmask_b32 v21, v[" #R "], v28, %[own]\n\t" \
      "v_xor_b32 v22, %[tag], v[" #R "+1]\n\t" \
      "v_xor_b32 v23, %[tag], v[" #R "+3]\n\t" \
      "v_mov_b32_dpp v20, v21 row_ror:8 row_mask:0xf bank_mask:0xc\n\t"     \
      "v_mov_b32_dpp v21, v21 row_ror:8 row_mask:0xf bank_mask:0x3\n\t"     \
      "v_bitop3_b32 v22, v22, v[" #R "+5], %[tag] bitop3:0xf6\n\t"          \
      "v_max_f32_dpp v18, v20, v20 quad_perm:[1,0,3,2]" DPPM \
      "v_bitop3_b32 v23, v23, v[" #R "+7], %[tag] bitop3:0xf6\n\t" \
      "v_bitop3_b32 v22, v22, v[" #R "+9], %[tag] bitop3:0xf6\n\t" \
      "v_max_f32_dpp v18, v18, v18 quad_perm:[2,3,0,1]" DPPM \
      "v_bitop3_b32 v23, v23, v[" #R "+11], %[tag] bitop3:0xf6\n\t" \
      "v_bitop3_b32 v22, v22, v[" #R "+13], %[tag] bitop3:0xf6\n\t" \
      "v_max_f32_dpp v18, v18, v18 row_half_mirror" DPPM \
      "v_bitop3_b32 v23, v23, v[" #R "+15], %[tag] bitop3:0xf6\n\t" \
      "v_sub_f32 v20, v20, v18\n\t" \
      "v_exp_f32 v20, v20\n\t"                                              \
      "v_or_b32 v22, v22, v23\n\t" \
      "v_cmp_ne_u32 vcc, 0, v22\n\t" \
      "v_mul_f32 v21, v20, v21\n\t" \
      "s_cbranch_vccnz " LBL "f\n\t" \
      "v_mul_f32_dpp v16, v20, v24 row_newbcast:0" DPPM \
      "v_mul_f32_dpp v17, v20, v[" #R "+2] row_newbcast:1" DPPM \
      "v_add_f32_dpp v21, v21, v21 quad_perm:[1,0,3,2]" DPPM \
      "v_fmac_f32_dpp v16, v20, v[" #R "+4] row_newbcast:2" DPPM \
      "v_fmac_f32_dpp v17, v20, v[" #R "+6] row_newbcast:3" DPPM \
      "v_add_f32_dpp v21, v21, v21 quad_perm:[2,3,0,1]" DPPM \
      "v_fmac_f32_dpp v16, v20, v[" #R "+8] row_newbcast:4" DPPM \
      "v_fmac_f32_dpp v17, v20, v[" #R "+10] row_newbcast:5" DPPM \
      "v_add_f32_dpp v21, v21, v21 row_half_mirror" DPPM \
      "v_fmac_f32_dpp v16, v20, v[" #R "+12] row_newbcast:6" DPPM \
      "v_fmac_f32_dpp v17, v20, v[" #R "+14] row_newbcast:7" DPPM \
      "v_rcp_f32 v21, v21\n\t" \
      "v_add_f32 v16, v16, v17\n\t" \
      "v_mul_f32 v16, v16, v21\n\t" \
      "v_cvt_pk_bf16_f32 v16, v16, v16\n\t" \
      "global_store_short %[vout], v16, %[sout] sc1\n\t" \
      "global_store_dword v30, %[tag], %[sep] sc1\n\t" \
      "s_endpgm\n\t" \
      LBL ":\n\t"
  static_assert(SLOT_B == 640 && W == 16 && NSPLIT == 8, "offsets in the asm");
  asm volatile(
      "ds_read_b64 v[26:27], %[vl]\n\t"
      "ds_read2st64_b32 v[0:1], %[vrec] offset1:2\n\t"
      "ds_read2st64_b32 v[2:3], %[vrec] offset0:4 offset1:6\n\t"
      "ds_read2st64_b32 v[4:5], %[vrec] offset0:8 offset1:10\n\t"
      "ds_read2st64_b32 v[6:7], %[vrec] offset0:12 offset1:14\n\t"
      "ds_read2st64_b32 v[8:9], %[vrec] offset0:16 offset1:18\n\t"
      "ds_read2st64_b32 v[10:11], %[vrec] offset0:20 offset1:22\n\t"
      "ds_read2st64_b32 v[12:13], %[vrec] offset0:24 offset1:26\n\t"
      "ds_read2st64_b32 v[14:15], %[vrec] offset0:28 offset1:30\n\t"
      "s_cmp_eq_u32 %[s], 0\n\t"
      "s_waitcnt lgkmcnt(8)\n\t"
      "v_max_f32_dpp v28, v26, v26 quad_perm:[1,0,3,2]" DPPM
      "v_mov_b32 v25, %[tag]\n\t"
      "v_mov_b32 v29, %[tag]\n\t"
      "v_max_f32_dpp v28, v28, v28 quad_perm:[2,3,0,1]" DPPM
      "v_mov_b32 v30, 0\n\t"
      "s_nop 0\n\t"
      "v_max_f32_dpp v28, v28, v28 row_half_mirror" DPPM
      "s_nop 1\n\t"
      "v_max_f32_dpp v28, v28, v28 row_mirror" DPPM
      "v_sub_f32 v26, v26, v28\n\t"
      "v_exp_f32 v26, v26\n\t"
      "s_waitcnt lgkmcnt(0)\n\t"
      "v_mul_f32 v27, v26, v27\n\t"
      "v_mul_f32_dpp v16, v26, v0 row_newbcast:0" DPPM
      "v_mul_f32_dpp v17, v26, v1 row_newbcast:1" DPPM
      "v_add_f32_dpp v27, v27, v27 quad_perm:[1,0,3,2]" DPPM
      "v_mul_f32_dpp v18, v26, v2 row_newbcast:2" DPPM
      "v_mul_f32_dpp v19, v26, v3 row_newbcast:3" DPPM
      "v_add_f32_dpp v27, v27, v27 quad_perm:[2,3,0,1]" DPPM
      "v_fmac_f32_dpp v16, v26, v4 row_newbcast:4" DPPM
      "v_fmac_f32_dpp v17, v26, v5 row_newbcast:5" DPPM
      "v_fmac_f32_dpp v18, v26, v6 row_newbcast:6" DPPM
      "v_fmac_f32_dpp v19, v26, v7 row_newbcast:7" DPPM
      "v_add_f32_dpp v27, v27, v27 row_half_mirror" DPPM
      "v_fmac_f32_dpp v16, v26, v8 row_newbcast:8" DPPM
      "v_fmac_f32_dpp v17, v26, v9 row_newbcast:9" DPPM
      "v_fmac_f32_dpp v18, v26, v10 row_newbcast:10" DPPM
      "v_fmac_f32_dpp v19, v26, v11 row_newbcast:11" DPPM
      "v_add_f32_dpp v27, v27, v27 row_mirror" DPPM
      "v_fmac_f32_dpp v16, v26, v12 row_newbcast:12" DPPM
      "v_fmac_f32_dpp v17, v26, v13 row_newbcast:13" DPPM
      "v_fmac_f32_dpp v18, v26, v14 row_newbcast:14" DPPM
      "v_fmac_f32_dpp v19, v26, v15 row_newbcast:15" DPPM
      "v_pk_add_f32 v[16:17], v[16:17], v[18:19]\n\t"
      "v_cndmask_b32 v28, v28, v27, %[hi8]\n\t"
      "v_add_f32 v24, v16, v17\n\t"
      "s_cbranch_scc1 2f\n\t"
      "global_store_dwordx2 %[vme], v[24:25], %[sb] offset:-2560 sc1\n\t"
      "global_store_dwordx2 %[vmy], v[28:29], %[sb] offset:-2560 sc1\n\t"
      "s_endpgm\n\t"
      "2:\n\t"
      "s_mov_b32 s44, 0x100000\n\t"
      "s_sleep " SWEEP_D "\n\t"
      "4:\n\t"
      "s_memrealtime s[42:43]\n\t"
      "s_waitcnt lgkmcnt(0)\n\t"
      "s_sub_u32 s42, s42, %[t0]\n\t"
      "s_cmpk_lt_u32 s42, %c[tx]\n\t"
      "s_cbranch_scc1 4b\n\t"
      A_SWEEP(0)
      "s_sleep " SWEEP_P "\n\t"
      A_SWEEP(32)
      "1:\n\t"
      A_TRY(0, "5") A_SWEEP(0)
      A_TRY(32, "6") A_SWEEP(32)
      "s_sub_u32 s44, s44, 1\n\t"
      "s_cbranch_scc0 1b\n\t"
      "s_endpgm"
      :
      : [vo] "v"(vo), [vme] "v"(vme), [vml] "v"(vml), [vmy] "v"(vmy), [vrec] "v"(vrec), [vl] "v"(vl), [vout] "v"(vout), [tag] "v"(tag),
        [sb] "s"(sb), [own] "s"(own), [hi8] "s"(hi8), [sout] "s"(out), [sep] "s"(hw), [s] "s"(s),
        [t0] "s"(t0), [tx] "i"((2 * W - 1) * PACE_NS / 10 + SWEEP_T)
      : "memory", "vcc", "scc", "s42", "s43", "s44", "v0", "v1", "v2", "v3", "v4", "v5", "v6", "v7", "v8", "v9", "v10", "v11", "v12", "v13",
        "v14", "v15", "v16", "v17", "v18", "v19", "v20", "v21", "v22", "v23", "v24", "v25", "v26", "v27", "v28", "v29", "v30",
        "v32", "v33", "v34", "v35", "v36", "v37", "v38", "v39", "v40", "v41", "v42", "v43", "v44", "v45", "v46", "v47");
}

extern "C" void kernel_launch(void* const* d_in, const int* in_sizes, int n_in, void* d_out, int out_size, void* d_ws,
                              size_t ws_size, hipStream_t stream) {
  attn_decode<<<H * NSPLIT, W * 64, 0, stream>>>(d_in[0], d_in[1], d_in[2], d_out, d_ws);
}
```
